```python
import torch

def _softmax(x, axis=-1):
    return torch.softmax(x, dim=axis)
def _gelu(x):
    return torch.nn.functional.gelu(x, approximate="tanh")
def _silu(x):
    return torch.nn.functional.silu(x)
def _sigmoid(x):
    return torch.sigmoid(x)
def _layernorm(x, axis=-1, eps=1e-5):
    m = x.mean(dim=axis, keepdim=True)
    v = x.var(dim=axis, keepdim=True, unbiased=False)
    return (x - m) / torch.sqrt(v + eps)
def _scatter_add(tgt, idx, src, axis=0):
    return tgt.clone().index_add_(axis, idx, src)
def _segment_sum(src, seg, n):
    out = torch.zeros((n,) + src.shape[1:], dtype=src.dtype)
    return out.index_add_(0, seg, src)
def _pad(x, pw):
    flat = tuple(v for pair in reversed(pw) for v in pair)
    return torch.nn.functional.pad(x, flat)

kernel_name = 'gated_mix__mha_block__geglu_ffn__prompt'

def setup_inputs():

    _w = (lambda a: (lambda t: t.to(torch.bfloat16) if t.is_floating_point() else t)(torch.as_tensor(a)))
    def _A_setup():
        B, S, D, H = 1, 2048, 1024, 32
        return {
            "x": _w((lambda *s: torch.randn(*s, dtype=torch.bfloat16) * 0.05)(B, S, D)),
            "wqkv": _w((lambda *s: torch.randn(*s, dtype=torch.bfloat16) * 0.8)(D, 3 * D)),
            "wo": _w((lambda *s: torch.randn(*s, dtype=torch.bfloat16) * 0.05)(D, D)),
        }
    _A_in = _A_setup()

    def _B_setup():
        B, S, D, F = 1, 2048, 1024, 3584
        return {
            "x": _w((lambda *s: torch.randn(*s, dtype=torch.bfloat16) * 0.05)(B, S, D)),
            "w_gate": _w((lambda *s: torch.randn(*s, dtype=torch.bfloat16) * 0.1)(D, F)),
            "w_up": _w((lambda *s: torch.randn(*s, dtype=torch.bfloat16) * 0.1)(D, F)),
            "w_down": _w((lambda *s: torch.randn(*s, dtype=torch.bfloat16) * 0.1)(F, D)),
        }
    _B_in = _B_setup()

    return {
        "A_x": _A_in["x"],
        "A_wqkv": _A_in["wqkv"],
        "A_wo": _A_in["wo"],
        "B_x": _B_in["x"],
        "B_w_gate": _B_in["w_gate"],
        "B_w_up": _B_in["w_up"],
        "B_w_down": _B_in["w_down"],
        "gate_w": _w((lambda *s: torch.randn(*s, dtype=torch.bfloat16) * 0.05)(1024) * 16.0),
    }

def reference(A_x, A_wqkv, A_wo, B_x, B_w_gate, B_w_up, B_w_down, gate_w):
    def _A_ref(x, wqkv, wo):
        B, S, D = x.shape
        H = 32
        Dh = D // H
        qkv = (lambda x, *d: x.permute(*d))((x @ wqkv).reshape(B, S, 3, H, Dh), 2, 0, 3, 1, 4)
        q, k, v = qkv[0], qkv[1], qkv[2]
        scores = torch.einsum("bhqd,bhkd->bhqk", q, k) * (Dh ** -0.5)
        p = _softmax(scores, axis=-1)
        o = torch.einsum("bhqk,bhkd->bhqd", p, v)
        return (lambda x, *d: x.permute(*d))(o, 0, 2, 1, 3).reshape(B, S, D) @ wo

    def _B_ref(x, w_gate, w_up, w_down):
        return (_gelu(x @ w_gate) * (x @ w_up)) @ w_down

    x = A_x
    g = _sigmoid((x * gate_w).sum(-1, keepdims=True))
    a = _A_ref(x, A_wqkv, A_wo)
    b = _B_ref(x, B_w_gate, B_w_up, B_w_down)
    return g * a + (1 - g) * b
```

```cpp
#include <hip/hip_runtime.h>
#include <hip/hip_bf16.h>
typedef unsigned short u16;
__device__ inline float bf2f(u16 b){ return __uint_as_float(((unsigned)b)<<16); }
__device__ inline u16 f2bf(float f){ unsigned u=__float_as_uint(f); u += 0x7FFFu + ((u>>16)&1u); return (u16)(u>>16); }

__global__ void gemm_naive(const u16* A, int lda, const u16* B, int ldb, u16* C, int ldc, int M, int N, int K){
  __shared__ float As[16][17], Bs[16][17];
  int tx=threadIdx.x, ty=threadIdx.y; int row=blockIdx.y*16+ty, col=blockIdx.x*16+tx;
  float acc=0.f;
  for(int k0=0;k0<K;k0+=16){
    As[ty][tx]=bf2f(A[(size_t)row*lda+k0+tx]);
    Bs[ty][tx]=bf2f(B[(size_t)(k0+ty)*ldb+col]);
    __syncthreads();
    #pragma unroll
    for(int k=0;k<16;k++) acc+=As[ty][k]*Bs[k][tx];
    __syncthreads();
  }
  C[(size_t)row*ldc+col]=f2bf(acc);
}
__global__ void attn_naive(const u16* qkv, u16* o){
  int h=blockIdx.y, q=blockIdx.x, t=threadIdx.x;
  __shared__ float sc[2048]; __shared__ float qs[32]; __shared__ float red[64];
  if(t<32) qs[t]=bf2f(qkv[(size_t)q*3072+h*32+t]);
  __syncthreads();
  float m=-1e30f;
  for(int k=t;k<2048;k+=64){ float s=0; for(int d=0;d<32;d++) s+=qs[d]*bf2f(qkv[(size_t)k*3072+1024+h*32+d]); s*=0.17677669529663687f; sc[k]=s; m=fmaxf(m,s);}
  red[t]=m; __syncthreads();
  for(int i=0;i<64;i++) m=fmaxf(m,red[i]);
  __syncthreads();
  float l=0; for(int k=t;k<2048;k+=64){ float e=__expf(sc[k]-m); sc[k]=e; l+=e; }
  red[t]=l; __syncthreads(); l=0; for(int i=0;i<64;i++) l+=red[i];
  __syncthreads();
  int d=t&31, half=t>>5; float acc=0;
  for(int k=half;k<2048;k+=2) acc+=sc[k]*bf2f(qkv[(size_t)k*3072+2048+h*32+d]);
  red[t]=acc; __syncthreads();
  if(t<32) o[(size_t)q*1024+h*32+t]=f2bf((red[t]+red[t+32])/l);
}
__global__ void geglu(const u16* g, const u16* u, u16* h, int n){
  int i=blockIdx.x*256+threadIdx.x; if(i<n){ float x=bf2f(g[i]); float t=0.7978845608028654f*(x+0.044715f*x*x*x); float ge=0.5f*x*(1.f+tanhf(t)); h[i]=f2bf(ge*bf2f(u[i])); }
}
__global__ void combine(const u16* x, const u16* gw, const u16* a, const u16* b, u16* out){
  int r=blockIdx.x, t=threadIdx.x; __shared__ float red[256];
  float s=0; for(int i=t;i<1024;i+=256) s+=bf2f(x[r*1024+i])*bf2f(gw[i]);
  red[t]=s; __syncthreads(); s=0; for(int i=0;i<256;i++) s+=red[i];
  float g=1.f/(1.f+__expf(-s));
  for(int i=t;i<1024;i+=256) out[r*1024+i]=f2bf(g*bf2f(a[r*1024+i])+(1.f-g)*bf2f(b[r*1024+i]));
}
extern "C" void kernel_launch(void* const* d_in, const int* in_sizes, int n_in, void* d_out, int out_size, void* d_ws, size_t ws_size, hipStream_t stream){
  const u16* x=(const u16*)d_in[0]; const u16* wqkv=(const u16*)d_in[1]; const u16* wo=(const u16*)d_in[2];
  const u16* wg=(const u16*)d_in[4]; const u16* wu=(const u16*)d_in[5]; const u16* wd=(const u16*)d_in[6]; const u16* gw=(const u16*)d_in[7];
  u16* ws=(u16*)d_ws;
  u16* qkv=ws; u16* o=qkv+2048*3072; u16* a=o+2048*1024; u16* G=a+2048*1024; u16* U=G+2048*3584; u16* H=U+2048*3584; u16* b=H+2048*3584;
  dim3 blk(16,16);
  gemm_naive<<<dim3(3072/16,128),blk,0,stream>>>(x,1024,wqkv,3072,qkv,3072,2048,3072,1024);
  attn_naive<<<dim3(2048,32),64,0,stream>>>(qkv,o);
  gemm_naive<<<dim3(1024/16,128),blk,0,stream>>>(o,1024,wo,1024,a,1024,2048,1024,1024);
  gemm_naive<<<dim3(3584/16,128),blk,0,stream>>>(x,1024,wg,3584,G,3584,2048,3584,1024);
  gemm_naive<<<dim3(3584/16,128),blk,0,stream>>>(x,1024,wu,3584,U,3584,2048,3584,1024);
  geglu<<<2048*3584/256,256,0,stream>>>(G,U,H,2048*3584);
  gemm_naive<<<dim3(1024/16,128),blk,0,stream>>>(H,3584,wd,1024,b,1024,2048,1024,3584);
  combine<<<2048,256,0,stream>>>(x,gw,a,b,(u16*)d_out);
}
```

```cpp
#define K1_AUX 2
#include <hip/hip_runtime.h>
#include <stdint.h>
typedef unsigned short u16;
typedef short bf16x8 __attribute__((ext_vector_type(8)));
typedef short s16x4 __attribute__((ext_vector_type(4)));
typedef float f32x4 __attribute__((ext_vector_type(4)));
typedef float f32x16 __attribute__((ext_vector_type(16)));
typedef unsigned u32x4 __attribute__((ext_vector_type(4)));
#define LAS __attribute__((address_space(3)))
typedef LAS const char* lds_cptr;
typedef short v4i16_t __attribute__((ext_vector_type(4)));
#define SBAR() __builtin_amdgcn_sched_barrier(0)

constexpr int S_ = 2048, D_ = 1024, F_ = 3584, NH_ = 32;
constexpr float QSCALE = 0.17677669529663687f * 1.4426950408889634f;

typedef __bf16 bf16x2_t __attribute__((ext_vector_type(2)));
typedef float f32x2_t __attribute__((ext_vector_type(2)));
__device__ __forceinline__ unsigned cvtpk(float lo, float hi) { const f32x2_t v = {lo, hi}; return __builtin_bit_cast(unsigned, __builtin_convertvector(v, bf16x2_t)); }
__device__ __forceinline__ void glds16s(const void* sbase, unsigned voff, unsigned lds_base) {
    asm volatile("s_mov_b32 m0, %2\n\ts_nop 0\n\tglobal_load_lds_dwordx4 %0, %1"
                 :: "v"(voff), "s"(sbase), "s"(lds_base) : "memory");
}
__device__ __forceinline__ s16x4 vtr(lds_cptr p) { return __builtin_bit_cast(s16x4, __builtin_amdgcn_ds_read_tr16_b64_v4i16((LAS v4i16_t*)p)); }
__device__ __forceinline__ bf16x8 cat8(s16x4 a, s16x4 b) { return (bf16x8){a[0], a[1], a[2], a[3], b[0], b[1], b[2], b[3]}; }
__device__ __forceinline__ bf16x8 ldsb128(lds_cptr p) { return *(const LAS bf16x8*)p; }

__device__ __forceinline__ lds_cptr LP(unsigned a) { return (lds_cptr)(uintptr_t)a; }
template <int X> __device__ __forceinline__ float swz_xor(float v) { return __int_as_float(__builtin_amdgcn_ds_swizzle(__float_as_int(v), (X << 10) | 0x1f)); }
__device__ __forceinline__ float add32(float v) { auto r = __builtin_amdgcn_permlane32_swap(__float_as_uint(v), __float_as_uint(v), false, false); return __uint_as_float(r[0]) + __uint_as_float(r[1]); }
__device__ __forceinline__ float max32(float v) { auto r = __builtin_amdgcn_permlane32_swap(__float_as_uint(v), __float_as_uint(v), false, false); return fmaxf(__uint_as_float(r[0]), __uint_as_float(r[1])); }
constexpr int K1_STAGE = 73728, K1_LDS = 2 * K1_STAGE;

__device__ __forceinline__ float gelu_mul(float g, float u) {
    const float t = g * g;
    const float p = __builtin_fmaf(t, -0.1029432397f, -2.302208198f);
    const float e = __builtin_amdgcn_exp2f(g * p);
    const float r = __builtin_amdgcn_rcpf(1.0f + e);
    return g * u * r;
}

__global__ __launch_bounds__(512, 2) void k1_gemm(const u16* __restrict__ x, const u16* __restrict__ wqkv, const u16* __restrict__ wg, const u16* __restrict__ wu,
                                                  const u16* __restrict__ gw, u16* __restrict__ QKV, u16* __restrict__ H, float* __restrict__ G, float* __restrict__ KMAX, unsigned* __restrict__ flags, unsigned long long* stamps)
{
    extern __shared__ __attribute__((aligned(16))) char lds[];
    const int tid = threadIdx.x, lane = tid & 63;
    const int wid = __builtin_amdgcn_readfirstlane(tid >> 6);
    const int wm = wid & 3, wn = wid >> 2;
    const int fr = lane & 15, fq = lane >> 4;
    const int bid = blockIdx.x, xcd = bid & 7, idx = bid >> 3, pm = idx & 7, pn = xcd * 4 + (idx >> 3);
    const bool typeB = pn >= 24;
    const unsigned lds0 = (unsigned)(uintptr_t)lds;
    unsigned long long ts0=0ull, tr0=0ull;

    if (bid == 0) { for (int i = tid; i < 264 * 32; i += 512) flags[i] = 0u; }
    const char* cb0; const char* cb1; const char* cb2; const char* cb3; const char* cb4;
    unsigned ldmid;
    if (!typeB) { cb0 = (const char*)(wg + 128 * pn); cb1 = (const char*)(wu + 128 * pn); cb2 = cb0 + 128; cb3 = cb1 + 128; cb4 = (const char*)(wqkv + 64 * pn); ldmid = F_ * 2; }
    else { const int i = pn - 24; cb0 = (const char*)(wg + 3072 + 64 * i); cb1 = (const char*)(wu + 3072 + 64 * i); cb2 = (const char*)(wqkv + 64 * (24 + 3 * i)); cb3 = cb2 + 128; cb4 = cb2 + 256; ldmid = 3072 * 2; }
    cb0 += (size_t)(8 * wid) * (F_ * 2); cb1 += (size_t)(8 * wid) * (F_ * 2); cb2 += (size_t)(8 * wid) * ldmid; cb3 += (size_t)(8 * wid) * ldmid; cb4 += (size_t)(8 * wid) * (3072 * 2);
    const char* ca = (const char*)x + (size_t)(256 * pm + 8 * wid) * 2048;
    const int sr = lane >> 3, spos = lane & 7;
    const unsigned fB = ((sr >> 1) & 1) | ((wid & 1) << 1);
    const unsigned colB = (((spos >> 1) ^ fB) * 32) + (spos & 1) * 16;
    const unsigned voffG = sr * (F_ * 2) + colB, voffM = sr * ldmid + colB, voffQ = sr * (3072 * 2) + colB;
    const unsigned voffA = sr * 2048 + ((spos ^ (4 * (wid & 1) + (sr >> 1))) * 16);
    const unsigned ldsw = lds0 + wid * 1024;
    float gsig;

#define K1_PIECE(i, sb) do { \
        if ((i) < 4) glds16s(ca + (size_t)(i) * 131072, voffA, (sb) + (i) * 8192); \
        else if ((i) == 4) glds16s(cb0, voffG, (sb) + 32768); \
        else if ((i) == 5) glds16s(cb2, voffM, (sb) + 32768 + 16384); \
        else if ((i) == 6) glds16s(cb1, voffG, (sb) + 32768 + 8192); \
        else if ((i) == 7) glds16s(cb3, voffM, (sb) + 32768 + 24576); \
        else glds16s(cb4, voffQ, (sb) + 32768 + 32768); } while (0)
#define K1_ADV1() do { ca += 128; cb0 += 64 * F_ * 2; cb2 += (size_t)64 * ldmid; } while (0)
#define K1_ADV2() do { cb1 += 64 * F_ * 2; cb3 += (size_t)64 * ldmid; cb4 += 64 * 3072 * 2; } while (0)

    const int grow = 256 * pm + 8 * pn + wid;
    u32x4 gx0, gx1, gg0, gg1;
    {
        const char* xr = (const char*)(x + (size_t)grow * D_);
        const unsigned go = lane * 32;
        asm volatile("global_load_dwordx4 %0, %4, %5\n\tglobal_load_dwordx4 %1, %4, %5 offset:16\n\tglobal_load_dwordx4 %2, %4, %6\n\tglobal_load_dwordx4 %3, %4, %6 offset:16"
                     : "=&v"(gx0), "=&v"(gx1), "=&v"(gg0), "=&v"(gg1) : "v"(go), "s"(xr), "s"(gw) : "memory");
    }
#pragma unroll
    for (int i = 0; i < 9; ++i) K1_PIECE(i, ldsw);
    K1_ADV1(); K1_ADV2();
#pragma unroll
    for (int i = 0; i < 7; ++i) K1_PIECE(i, ldsw + K1_STAGE);
    K1_ADV1();
    unsigned long long tsa=0ull;
    asm volatile("s_waitcnt vmcnt(16)" : "+v"(gx0), "+v"(gx1), "+v"(gg0), "+v"(gg1) :: "memory");
    {
        float s = 0.f;
#pragma unroll
        for (int j = 0; j < 4; ++j) {
            s += __uint_as_float(gx0[j] << 16) * __uint_as_float(gg0[j] << 16) + __uint_as_float(gx0[j] & 0xffff0000u) * __uint_as_float(gg0[j] & 0xffff0000u);
            s += __uint_as_float(gx1[j] << 16) * __uint_as_float(gg1[j] << 16) + __uint_as_float(gx1[j] & 0xffff0000u) * __uint_as_float(gg1[j] & 0xffff0000u);
        }
        s += swz_xor<1>(s); s += swz_xor<2>(s); s += swz_xor<4>(s); s += swz_xor<8>(s); s += swz_xor<16>(s); s = add32(s);
        gsig = 1.0f / (1.0f + __expf(-s));
    }

    unsigned long long tsb=0ull;
    unsigned aA0 = lds0 + (64 * wm + fr) * 128 + (((0 + fq) ^ (fr >> 1)) * 16);
    unsigned aA1 = lds0 + (64 * wm + fr) * 128 + (((4 + fq) ^ (fr >> 1)) * 16);
    const int tq = (lane & 15) >> 2, tp = lane & 3;
    const unsigned fR = ((tq >> 1) & 1) | ((fq & 1) << 1);
    const unsigned bbase = lds0 + 32768 + (8 * fq + tq) * 128 + 8 * tp;
    unsigned aB0 = bbase + 2 * wn * 8192 + ((0 ^ fR) << 5);
    unsigned aB1 = bbase + 2 * wn * 8192 + ((1 ^ fR) << 5);
    unsigned aB2 = bbase + 2 * wn * 8192 + ((2 ^ fR) << 5);
    unsigned aB3 = bbase + 2 * wn * 8192 + ((3 ^ fR) << 5);
    unsigned aB4a = bbase + 4 * 8192 + (((2 * wn) ^ fR) << 5);
    unsigned aB4b = bbase + 4 * 8192 + (((2 * wn + 1) ^ fR) << 5);

    f32x4 acc[4][10];
#pragma unroll
    for (int m = 0; m < 4; ++m)
#pragma unroll
        for (int s = 0; s < 10; ++s) acc[m][s] = (f32x4){0.f, 0.f, 0.f, 0.f};
    bf16x8 fa0[4], fa1[4];
    s16x4 bx[4], by[4];

#define MF(fb, h, fa, m, ti) acc[m][(ti) + (h)] = __builtin_amdgcn_mfma_f32_16x16x32_bf16(cat8(fb[2 * (h)], fb[2 * (h) + 1]), fa[m], acc[m][(ti) + (h)], 0, 0, 0)
#define RB(dst, j, aa, ab, off) dst[j] = vtr(LP(((j) < 2) ? (aa) : (ab)) + (off) + ((j) & 1) * 512)
#define RA(dst, m, a) dst[m] = ldsb128(LP(a) + (m) * 2048)
#define SUBP(cur, fa, ti, nxt, aa, ab, off, AR, GL) do { \
        MF(cur, 0, fa, 0, ti); SBAR(); RB(nxt, 0, aa, ab, off); SBAR(); \
        MF(cur, 1, fa, 0, ti); SBAR(); RB(nxt, 1, aa, ab, off); SBAR(); \
        MF(cur, 0, fa, 1, ti); SBAR(); RB(nxt, 2, aa, ab, off); SBAR(); \
        MF(cur, 1, fa, 1, ti); SBAR(); RB(nxt, 3, aa, ab, off); SBAR(); \
        MF(cur, 0, fa, 2, ti); SBAR(); AR(0); GL; SBAR(); \
        MF(cur, 1, fa, 2, ti); SBAR(); AR(1); SBAR(); \
        MF(cur, 0, fa, 3, ti); SBAR(); AR(2); SBAR(); \
        MF(cur, 1, fa, 3, ti); SBAR(); AR(3); SBAR(); } while (0)
#define NOA(m) do { } while (0)
#define RA0(m) RA(fa0, m, aA0)
#define RA1(m) RA(fa1, m, aA1)
#define NOG do { } while (0)
#define WBAR(n) asm volatile("s_waitcnt vmcnt(" #n ") lgkmcnt(0)\n\ts_barrier" ::: "memory")

    K1_PIECE(6, ldsw + K1_STAGE);
    asm volatile("s_waitcnt vmcnt(10)\n\ts_barrier" ::: "memory");
    if (lane == 0) G[grow] = gsig;
#pragma unroll
    for (int j = 0; j < 4; ++j) RB(bx, j, aB0, aB1, 0);
#pragma unroll
    for (int m = 0; m < 4; ++m) RA0(m);
    int delta = K1_STAGE;
    unsigned long long ts1=0ull; tsa=0ull;

#define VADD(a) asm volatile("v_add_u32 %0, %1, %0" : "+v"(a) : "s"(delta))
#define STEP(MAIN) do { \
        SUBP(bx, fa0, 0, by, aB0, aB1, 4096, RA1, if (MAIN || p2) K1_PIECE(7, ns)); \
        SUBP(by, fa1, 0, bx, aB2, aB3, 0, NOA, if (MAIN || p2) { K1_PIECE(8, ns); K1_ADV2(); }); \
        SUBP(bx, fa0, 2, by, aB2, aB3, 4096, NOA, NOG); \
        if (MAIN || p2) WBAR(9); else WBAR(0); \
        SUBP(by, fa1, 2, bx, aB0, aB1, 8192, NOA, if (MAIN) K1_PIECE(0, cs)); \
        SUBP(bx, fa0, 4, by, aB0, aB1, 8192 + 4096, NOA, if (MAIN) K1_PIECE(1, cs)); \
        SUBP(by, fa1, 4, bx, aB2, aB3, 8192, NOA, if (MAIN) K1_PIECE(2, cs)); \
        SUBP(bx, fa0, 6, by, aB2, aB3, 8192 + 4096, NOA, if (MAIN) K1_PIECE(3, cs)); \
        SUBP(by, fa1, 6, bx, aB4a, aB4b, 0, NOA, if (MAIN) K1_PIECE(4, cs)); \
        SUBP(bx, fa0, 8, by, aB4a, aB4b, 4096, NOA, if (MAIN) { K1_PIECE(5, cs); K1_ADV1(); }); \
        VADD(aA0); VADD(aA1); VADD(aB0); VADD(aB1); VADD(aB2); VADD(aB3); \
        if (MAIN) WBAR(9); else WBAR(3); \
        SUBP(by, fa1, 8, bx, aB0, aB1, 0, RA0, if (MAIN) K1_PIECE(6, cs)); \
        VADD(aB4a); VADD(aB4b); delta = -delta; { const unsigned tmp = cs; cs = ns; ns = tmp; } \
    } while (0)
    unsigned cs = ldsw, ns = ldsw + K1_STAGE;
#pragma nounroll
    for (int tt = 0; tt < 14; ++tt) { const bool p2 = true; STEP(true); asm volatile("" : "+s"(cs), "+s"(ns), "+s"(delta)); }
#pragma nounroll
    for (int tt = 0; tt < 2; ++tt) { int t = tt; asm volatile("" : "+s"(t)); const bool p2 = t == 0; STEP(false); asm volatile("" : "+s"(cs), "+s"(ns), "+s"(delta)); }
    unsigned long long ts2=0ull; tsb=0ull;

    const int row0 = 256 * pm + 64 * wm;
    const __amdgpu_buffer_rsrc_t rsH = __builtin_amdgcn_make_buffer_rsrc(H, 0, 2048 * 3584 * 2, 0x00020000), rsQ = __builtin_amdgcn_make_buffer_rsrc(QKV, 0, 96 * 2048 * 64, 0x00020000);
#define ST16(base, rs, off, w) __builtin_amdgcn_raw_buffer_store_b128(w, rs, off, 0, K1_AUX)
    const unsigned swz = ((fq ^ ((-(fr >> 2)) & 3)) * 16) + fr * 64;
    const bool gu = !typeB || wn == 0;
    if (gu) {
        const int hc0 = typeB ? (3072 + 64 * (pn - 24)) : (128 * pn + 64 * wn);
#pragma unroll
        for (int m = 0; m < 4; ++m) {
            const int mt = (row0 >> 4) + m;
#pragma unroll
            for (int i = 0; i < 2; ++i) {
                u32x4 w;
                { const f32x4 g = acc[m][2 * i], u = acc[m][4 + 2 * i];
                  w[0] = cvtpk(gelu_mul(g[0], u[0]), gelu_mul(g[1], u[1])); w[1] = cvtpk(gelu_mul(g[2], u[2]), gelu_mul(g[3], u[3])); }
                { const f32x4 g = acc[m][2 * i + 1], u = acc[m][4 + 2 * i + 1];
                  w[2] = cvtpk(gelu_mul(g[0], u[0]), gelu_mul(g[1], u[1])); w[3] = cvtpk(gelu_mul(g[2], u[2]), gelu_mul(g[3], u[3])); }
                ST16(H, rsH, ((unsigned)mt * 112 + (hc0 >> 5) + i) * 1024 + swz, w);
            }
        }
    }
    auto store_head = [&](int hb, const f32x4 (&ta)[4], const f32x4 (&tb)[4]) {
        const int which = hb >> 5;
        const float sc = which == 0 ? QSCALE : 1.0f;
        float kn = 0.f;
#pragma unroll
        for (int m = 0; m < 4; ++m) {
            const f32x4 a = ta[m] * sc, b = tb[m] * sc;
            u32x4 w; w[0] = cvtpk(a[0], a[1]); w[1] = cvtpk(a[2], a[3]); w[2] = cvtpk(b[0], b[1]); w[3] = cvtpk(b[2], b[3]);
            ST16(QKV, rsQ, ((unsigned)hb * 2048 + row0 + 16 * m + fr) * 64 + fq * 16, w);
            if (which == 1) {
                float n2 = a[0] * a[0] + a[1] * a[1] + a[2] * a[2] + a[3] * a[3] + b[0] * b[0] + b[1] * b[1] + b[2] * b[2] + b[3] * b[3];
                n2 += swz_xor<16>(n2); n2 = add32(n2);
                kn = fmaxf(kn, n2);
            }
        }
        if (which == 1) {
            kn = fmaxf(kn, swz_xor<8>(kn)); kn = fmaxf(kn, swz_xor<4>(kn)); kn = fmaxf(kn, swz_xor<2>(kn)); kn = fmaxf(kn, swz_xor<1>(kn));
            if (lane == 0) KMAX[(hb & 31) * 32 + pm * 4 + wm] = kn;
        }
    };
    {
        const int qc4 = typeB ? (24 + 3 * (pn - 24) + 2) : pn;
        const f32x4 ta[4] = {acc[0][8], acc[1][8], acc[2][8], acc[3][8]};
        const f32x4 tb[4] = {acc[0][9], acc[1][9], acc[2][9], acc[3][9]};
        store_head(2 * qc4 + wn, ta, tb);
    }
    if (!gu) {
        const int qc2 = 24 + 3 * (pn - 24);
#pragma unroll
        for (int c = 0; c < 2; ++c)
#pragma unroll
            for (int hh = 0; hh < 2; ++hh) {
                const f32x4 ta[4] = {acc[0][4 * c + 2 * hh], acc[1][4 * c + 2 * hh], acc[2][4 * c + 2 * hh], acc[3][4 * c + 2 * hh]};
                const f32x4 tb[4] = {acc[0][4 * c + 2 * hh + 1], acc[1][4 * c + 2 * hh + 1], acc[2][4 * c + 2 * hh + 1], acc[3][4 * c + 2 * hh + 1]};
                store_head(2 * (qc2 + c) + hh, ta, tb);
            }
    }
}

#define MFMA32(a, b, c) __builtin_amdgcn_mfma_f32_32x32x16_bf16(a, b, c, 0, 0, 0)
#define MFMA16(a, b, c) __builtin_amdgcn_mfma_f32_16x16x32_bf16(a, b, c, 0, 0, 0)
constexpr int K2_SLOT = 16384, K2_NS = 4, K2_LDS = K2_SLOT * K2_NS;

__device__ __forceinline__ bf16x8 pack8(const f32x16& p, int b) {
    u32x4 w; w[0] = cvtpk(p[b], p[b + 1]); w[1] = cvtpk(p[b + 2], p[b + 3]); w[2] = cvtpk(p[b + 4], p[b + 5]); w[3] = cvtpk(p[b + 6], p[b + 7]);
    return __builtin_bit_cast(bf16x8, w);
}

#define K2_ISSUE(st) do { glds16s(Kh + (size_t)(st) * 8192, voffK, ldsw + ((st) & 3) * K2_SLOT); glds16s(Vh + (size_t)(st) * 8192, voffV, ldsw + ((st) & 3) * K2_SLOT + 8192); } while (0)
#define RDK(d0, d1, slot, i) do { d0 = ldsb128(LP(aK0 + (slot)) + (i) * 2048); d1 = ldsb128(LP(aK1 + (slot)) + (i) * 2048); } while (0)
#define QK(S, k0, k1) do { S = MFMA32(k0, qf0, negm); S = MFMA32(k1, qf1, S); } while (0)
#define PIN(x) asm volatile("" : "+v"(x))
#define EX4(S, b) do { S[b] = __builtin_amdgcn_exp2f(S[b]); S[b + 1] = __builtin_amdgcn_exp2f(S[b + 1]); S[b + 2] = __builtin_amdgcn_exp2f(S[b + 2]); S[b + 3] = __builtin_amdgcn_exp2f(S[b + 3]); } while (0)
#define RDV(d, slot, i) do { d[0] = vtr(LP(aV + (slot)) + (i) * 2048); d[1] = vtr(LP(aV + (slot)) + (i) * 2048 + 512); d[2] = vtr(LP(aV + (slot)) + (i) * 2048 + 1024); d[3] = vtr(LP(aV + (slot)) + (i) * 2048 + 1536); } while (0)
#define ITER(SC, SN, k0, k1, n0, n1, vc, vn, kslot, ki, vslot, vi) do { \
        SN = MFMA32(k0, qf0, negm); SBAR(); \
        EX4(SC, 0); PIN(SC); SBAR(); \
        SN = MFMA32(k1, qf1, SN); SBAR(); \
        RDK(n0, n1, kslot, ki); SBAR(); \
        EX4(SC, 4); const bf16x8 p0 = pack8(SC, 0); SBAR(); \
        o = MFMA32(cat8(vc[0], vc[1]), p0, o); SBAR(); \
        EX4(SC, 8); PIN(SC); SBAR(); \
        lsum = MFMA16(sel, p0, lsum); SBAR(); \
        EX4(SC, 12); const bf16x8 p1 = pack8(SC, 8); SBAR(); \
        o = MFMA32(cat8(vc[2], vc[3]), p1, o); SBAR(); \
        RDV(vn, vslot, vi); SBAR(); \
        lsum2 = MFMA16(sel, p1, lsum2); SBAR(); \
    } while (0)
#define WB2(n) asm volatile("s_waitcnt vmcnt(" #n ") lgkmcnt(0)\n\ts_barrier" ::: "memory")
constexpr int K23_LDS = 81920;
#define GEMM_PRIO 0
#define ATT_PRIO 0

__device__ __forceinline__ void attn_role(const u16* __restrict__ QKV, const float* __restrict__ KMAX, const float* __restrict__ G, u16* __restrict__ O, unsigned* __restrict__ cnt, char* lds, int bid, unsigned long long* stamps)
{
    const int tid = threadIdx.x, lane = tid & 63;
    const int wid = __builtin_amdgcn_readfirstlane(tid >> 6);
    const int r32 = lane & 31, hi = lane >> 5;
    const int xcd = bid & 7, idx = bid >> 3, head = 4 * xcd + (idx >> 3), qb = idx & 7;
    const unsigned lds0 = (unsigned)(uintptr_t)lds;
    unsigned long long ts0 = 0ull, tr0 = 0ull;
    if (ATT_PRIO) __builtin_amdgcn_s_setprio(ATT_PRIO);
    const char* Qh = (const char*)QKV + (size_t)head * 131072;
    const char* Kh = (const char*)QKV + (size_t)(32 + head) * 131072 + wid * 1024;
    const char* Vh = (const char*)QKV + (size_t)(64 + head) * 131072 + wid * 1024;
    const unsigned voffK = (lane >> 2) * 64 + (((lane & 3) ^ ((lane >> 4) & 3)) * 16), voffV = lane * 16;
    const unsigned ldsw = lds0 + wid * 1024;
    K2_ISSUE(0); K2_ISSUE(1);
    const int q0 = qb * 256 + wid * 32;
    const bf16x8 qf0 = *(const bf16x8*)(Qh + (size_t)(q0 + r32) * 64 + hi * 16);
    const bf16x8 qf1 = *(const bf16x8*)(Qh + (size_t)(q0 + r32) * 64 + 32 + hi * 16);
    const float kmx = KMAX[head * 32 + r32];
    float qn2 = 0.f;
#pragma unroll
    for (int j = 0; j < 8; ++j) { const float a = __uint_as_float((unsigned)(u16)qf0[j] << 16), b = __uint_as_float((unsigned)(u16)qf1[j] << 16); qn2 += a * a + b * b; }
    qn2 = add32(qn2);
    float km2 = kmx; km2 = fmaxf(km2, swz_xor<1>(km2)); km2 = fmaxf(km2, swz_xor<2>(km2)); km2 = fmaxf(km2, swz_xor<4>(km2)); km2 = fmaxf(km2, swz_xor<8>(km2)); km2 = fmaxf(km2, swz_xor<16>(km2));
    const float mb = sqrtf(qn2 * km2) * 1.01f;
    f32x16 negm;
#pragma unroll
    for (int r = 0; r < 16; ++r) negm[r] = -mb;
    asm volatile("" : "+v"(negm));
    bf16x8 sel;
    { const short one = ((lane & 15) == ((lane >> 4) & 1)) ? (short)0x3F80 : (short)0;
#pragma unroll
      for (int j = 0; j < 8; ++j) sel[j] = one; }
    const unsigned aK0 = lds0 + r32 * 64 + (((0 + hi) ^ ((r32 >> 2) & 3)) * 16);
    const unsigned aK1 = lds0 + r32 * 64 + (((2 + hi) ^ ((r32 >> 2) & 3)) * 16);
    const unsigned aV = lds0 + 8192 + (4 * hi + ((lane & 15) >> 2)) * 64 + ((lane >> 4) & 1) * 32 + (lane & 3) * 8;
    f32x16 o = {}; f32x4 lsum = {0.f, 0.f, 0.f, 0.f}, lsum2 = {0.f, 0.f, 0.f, 0.f};
    f32x16 sA, sB;
    bf16x8 kfa0, kfa1, kfb0, kfb1;
    s16x4 va[4], vb[4];
    WB2(2);
    K2_ISSUE(2);
    RDK(kfa0, kfa1, 0, 0); RDK(kfb0, kfb1, 0, 1); RDV(va, 0, 0);
    QK(sA, kfa0, kfa1);
    unsigned long long ts1 = 0ull;
#pragma nounroll
    for (int st = 0; st < 16; ++st) {
        int stv = st; asm volatile("" : "+s"(stv));
        const unsigned cur = (stv & 3) * K2_SLOT, nxt = ((stv + 1) & 3) * K2_SLOT;
        ITER(sA, sB, kfb0, kfb1, kfa0, kfa1, va, vb, cur, 2, cur, 1);
        ITER(sB, sA, kfa0, kfa1, kfb0, kfb1, vb, va, cur, 3, cur, 2);
        if (stv <= 12) { WB2(2); K2_ISSUE(stv + 3); } else if (stv == 13) WB2(2); else WB2(0);
        ITER(sA, sB, kfb0, kfb1, kfa0, kfa1, va, vb, nxt, 0, cur, 3);
        ITER(sB, sA, kfa0, kfa1, kfb0, kfb1, vb, va, nxt, 1, nxt, 0);
    }
    lsum += lsum2;
    unsigned long long ts2 = 0ull;
    int lz = threadIdx.x & 63; asm volatile("" : "+v"(lz));
    const int rr = lz & 31, hh = lz >> 5;
    const float gq = G[q0 + rr];
    const float l0 = __int_as_float(__builtin_amdgcn_ds_bpermute((rr & 15) << 2, __float_as_int(lsum[0]))), l1 = __int_as_float(__builtin_amdgcn_ds_bpermute((rr & 15) << 2, __float_as_int(lsum[1])));
    const float l = (rr & 16) ? l1 : l0;
    const float sc = gq / l;
    const int row = q0 + rr, fr = row & 15, mt = row >> 4;
    const __amdgpu_buffer_rsrc_t rs = __builtin_amdgcn_make_buffer_rsrc(O, 0, 2048 * 1024 * 2, 0x00020000);
    const unsigned ob = (unsigned)(mt * 32 + head) * 1024 + fr * 64;
    const int gsw = (-(fr >> 2)) & 3;
#pragma unroll
    for (int c = 0; c < 4; c += 2) {
        unsigned ax = cvtpk(o[4 * c] * sc, o[4 * c + 1] * sc), ay = cvtpk(o[4 * c + 2] * sc, o[4 * c + 3] * sc);
        unsigned bx = cvtpk(o[4 * c + 4] * sc, o[4 * c + 5] * sc), by = cvtpk(o[4 * c + 6] * sc, o[4 * c + 7] * sc);
        auto rx = __builtin_amdgcn_permlane32_swap(ax, bx, false, false); auto ry = __builtin_amdgcn_permlane32_swap(ay, by, false, false);
        const u32x4 w = {rx[0], ry[0], rx[1], ry[1]};
        __builtin_amdgcn_raw_buffer_store_b128(w, rs, ob + (((c + hh) ^ gsw) * 16), 0, 16);
    }
    asm volatile("s_waitcnt vmcnt(0)" ::: "memory");
    if (lz == 0) __hip_atomic_fetch_add(cnt + qb * 32, 1u, __ATOMIC_RELAXED, __HIP_MEMORY_SCOPE_AGENT);
}

#define G_RD(x, w, so, s) do { \
        x[0] = ldsb128(LP((aX0 + (so)) ^ ((s) * 32))); w[0] = vtr(LP(aW0 + (so)) + (s) * 2048); w[1] = vtr(LP(aW0 + (so)) + (s) * 2048 + 512); \
        x[1] = ldsb128(LP((aX0 + (so)) ^ ((s) * 32)) + 2048); w[2] = vtr(LP((aW0 + (so)) ^ 64) + (s) * 2048); w[3] = vtr(LP((aW0 + (so)) ^ 64) + (s) * 2048 + 512); } while (0)
#define G_MM(x, w) do { \
        acc[0][0] = MFMA32(cat8(w[0], w[1]), x[0], acc[0][0]); acc[1][0] = MFMA32(cat8(w[0], w[1]), x[1], acc[1][0]); \
        acc[0][1] = MFMA32(cat8(w[2], w[3]), x[0], acc[0][1]); acc[1][1] = MFMA32(cat8(w[2], w[3]), x[1], acc[1][1]); } while (0)
#define DBGADV pa += 1024; pb += 32 * 2048;
#define PFD 6
__device__ __forceinline__ void gemm_role(const u16* __restrict__ H, const u16* __restrict__ Ob, const u16* __restrict__ wd, const u16* __restrict__ wo, const float* __restrict__ G,
                                          u16* __restrict__ out, float* __restrict__ P, unsigned* __restrict__ flags, unsigned* __restrict__ cnt, char* lds, int bid, unsigned long long* stamps)
{
    const int tid = threadIdx.x, lane = tid & 63;
    const int wid = __builtin_amdgcn_readfirstlane(tid >> 6);
    const int jb = bid - 256, xcd = jb & 7, idx = jb >> 3;
    const int ks = xcd & 1, pnl = idx >> 3, pml = idx & 7, pn = 4 * ((xcd >> 1) & 1) + pnl, pm = 8 * (xcd >> 2) + pml;
    const int tile = pm * 8 + pn;
    const unsigned lds0 = (unsigned)(uintptr_t)lds;
    unsigned long long ts0 = 0ull, tr0 = 0ull, ts1 = 0, ts2 = 0, tra = 0, trb = 0;
    if (GEMM_PRIO) __builtin_amdgcn_s_setprio(GEMM_PRIO);
#define VW(n) asm volatile("s_waitcnt vmcnt(" #n ") lgkmcnt(0)\n\ts_barrier" ::: "memory")
    const int sr = lane >> 3, spos = lane & 7;
    const unsigned voffB = ((sr & 3) + 16 * (sr >> 2)) * 2048 + ((spos ^ (((sr >> 1) & 1) << 2)) * 16);
    const unsigned voffA = lane * 16;
    const unsigned ldsA = lds0 + wid * 1024, ldsB = lds0 + 8192 + wid * 1024;
    const char* paH = (const char*)H + ((size_t)(pm * 8 + wid) * 112 + 56 * ks) * 1024;
    const char* pbH = (const char*)wd + (size_t)(56 * ks * 32 + 4 * (wid & 3)) * 2048 + pn * 256 + (wid >> 2) * 128;
    const char* paO = (const char*)Ob + ((size_t)(pm * 8 + wid) * 32 + 16 * ks) * 1024;
    const char* pbO = (const char*)wo + (size_t)(16 * ks * 32 + 4 * (wid & 3)) * 2048 + pn * 256 + (wid >> 2) * 128;
#define K4_ISSUE() do { const unsigned so_ = slot_i * 16384; glds16s(pa, voffA, ldsA + so_); glds16s(pb, voffB, ldsB + so_); DBGADV slot_i = (slot_i == 4) ? 0 : slot_i + 1; } while (0)
#define YBAR(jv, nh_) do { const int rem_ = (nh_) - 2 - (jv); \
        if ((jv) == 0) VW(3); else if ((jv) == 1) VW(4); else if ((jv) == 2) VW(5); else if (rem_ >= 3) VW(6); else if (rem_ == 2) VW(4); else if (rem_ == 1) VW(2); else VW(0); } while (0)
    if (wid >= 4) {
#define H_LOOP(nt_) do { \
            int slot_i = 0; \
            VW(0); \
            _Pragma("nounroll") for (int t = 0; t < (nt_) - 3; ++t) { asm volatile("" : "+s"(slot_i)); VW(6); K4_ISSUE(); VW(6); K4_ISSUE(); } \
            VW(6); K4_ISSUE(); VW(6); VW(4); VW(2); VW(0); VW(0); \
            asm volatile("s_barrier" ::: "memory"); } while (0)
        {
            const char* pa = paH; const char* pb = pbH;
#pragma unroll
            for (int i = 0; i < 5; ++i) { glds16s(pa + i * 1024, voffA, ldsA + i * 16384); glds16s(pb + (size_t)i * 32 * 2048, voffB, ldsB + i * 16384); }
            pa += 5 * 1024; pb += (size_t)5 * 32 * 2048;
            { int slot_i = 0;
              VW(8);
              _Pragma("nounroll") for (int t = 0; t < 28 - 3; ++t) { asm volatile("" : "+s"(slot_i)); VW(6); K4_ISSUE(); VW(6); K4_ISSUE(); }
              VW(6); K4_ISSUE(); VW(6); VW(4); VW(2); VW(0); VW(0);
              asm volatile("s_barrier" ::: "memory"); }
        }
        {
            const char* pa = paO; const char* pb = pbO;
#pragma unroll
            for (int i = 0; i < 5; ++i) glds16s(pb + (size_t)i * 32 * 2048, voffB, ldsB + i * 16384);
            pb += (size_t)5 * 32 * 2048;
            asm volatile("s_barrier" ::: "memory");
#pragma unroll
            for (int i = 0; i < 5; ++i) glds16s(pa + i * 1024, voffA, ldsA + i * 16384);
            pa += 5 * 1024;
            H_LOOP(8);
        }
        asm volatile("s_barrier" ::: "memory");
        return;
    }
    const int wm = wid & 1, wn = wid >> 1;
    const int r32 = lane & 31, hi = lane >> 5, fr = lane & 15;
    const unsigned aX0 = lds0 + (wm * 4 + (r32 >> 4)) * 1024 + fr * 64 + ((hi ^ ((-(fr >> 2)) & 3)) * 16);
    const int tq = (lane & 15) >> 2, tp = lane & 3;
    const unsigned aW0 = lds0 + 8192 + wn * 4096 + (8 * hi + tq) * 128 + (((tq >> 1) & 1) << 6) + ((lane >> 4) & 1) * 32 + tp * 8;
    f32x16 acc[2][2];
#pragma unroll
    for (int m = 0; m < 2; ++m)
#pragma unroll
        for (int n = 0; n < 2; ++n) acc[m][n] = (f32x16){};
    bf16x8 xp[2] = {}, xq[2] = {}; s16x4 wp[4] = {}, wq[4] = {};
    unsigned dummy = 0; unsigned long long tA0, tA1, tB0, tB1;
#define G_STEP(W1, I1, W2, I2) do { \
            const unsigned s0 = rs_i * 16384; \
            rs_i = (rs_i == 4) ? 0 : rs_i + 1; \
            const unsigned s1 = rs_i * 16384; \
            rs_i = (rs_i == 4) ? 0 : rs_i + 1; \
            const unsigned sn = rs_i * 16384; \
            G_RD(xq, wq, s0, 1); SBAR(); G_MM(xp, wp); SBAR(); \
            VW(W1); \
            G_RD(xp, wp, s1, 0); SBAR(); if (I1) K4_ISSUE(); SBAR(); G_MM(xq, wq); SBAR(); \
            G_RD(xq, wq, s1, 1); SBAR(); G_MM(xp, wp); SBAR(); \
            VW(W2); \
            G_RD(xp, wp, sn, 0); SBAR(); if (I2) K4_ISSUE(); SBAR(); G_MM(xq, wq); SBAR(); } while (0)
#define G_LOOP(nt_, W0, TSA, TSB) do { \
        int slot_i = 0, rs_i = 0; \
        VW(W0); \
        TSA = 0ull; \
        G_RD(xp, wp, 0, 0); \
        _Pragma("nounroll") for (int t = 0; t < (nt_) - 3; ++t) { asm volatile("" : "+s"(rs_i), "+s"(slot_i)); G_STEP(6, true, 6, true); } \
        G_STEP(6, true, 6, false); G_STEP(4, false, 2, false); G_STEP(0, false, 0, false); \
        asm volatile("s_waitcnt lgkmcnt(0)\n\ts_barrier" ::: "memory"); TSB = 0ull; } while (0)
    {
        const char* pa = paH; const char* pb = pbH;
#pragma unroll
        for (int i = 0; i < 5; ++i) { glds16s(pa + i * 1024, voffA, ldsA + i * 16384); glds16s(pb + (size_t)i * 32 * 2048, voffB, ldsB + i * 16384); }
        pa += 5 * 1024; pb += (size_t)5 * 32 * 2048;
        ts1 = 0ull;
        G_LOOP(28, 8, tA0, tA1);
    }
    {
        const char* pa = paO; const char* pb = pbO;
        int lz = threadIdx.x & 63; asm volatile("" : "+v"(lz));
        const float gm0 = 1.0f - G[pm * 128 + wm * 64 + (lz & 31)], gm1 = 1.0f - G[pm * 128 + wm * 64 + 32 + (lz & 31)];
        acc[0][0] *= gm0; acc[0][1] *= gm0; acc[1][0] *= gm1; acc[1][1] *= gm1;
#pragma unroll
        for (int i = 0; i < 5; ++i) glds16s(pb + (size_t)i * 32 * 2048, voffB, ldsB + i * 16384);
        pb += (size_t)5 * 32 * 2048;
        tra = 0ull;
        if (wid == 0) {
            unsigned spins = 0;
            while (__builtin_amdgcn_readfirstlane(__hip_atomic_load(cnt + (pm >> 1) * 32, __ATOMIC_RELAXED, __HIP_MEMORY_SCOPE_AGENT)) < 256u) { __builtin_amdgcn_s_sleep(2); if (++spins > (1u << 22)) break; }
            __builtin_amdgcn_fence(__ATOMIC_ACQUIRE, "agent");
            asm volatile("s_waitcnt vmcnt(0)" ::: "memory");
        }
        asm volatile("s_barrier" ::: "memory");
        trb = 0ull;
#pragma unroll
        for (int i = 0; i < 5; ++i) glds16s(pa + i * 1024, voffA, ldsA + i * 16384);
        pa += 5 * 1024;
        G_LOOP(8, 0, tB0, tB1);
    }
    ts2 = 0ull;
    {
        int lz = threadIdx.x & 63; asm volatile("" : "+v"(lz));
        const int rr = lz & 31, hh = lz >> 5;
        const __amdgpu_buffer_rsrc_t rs = __builtin_amdgcn_make_buffer_rsrc(P, 0, 128 * 2 * 32768, 0x00020000);
        const unsigned pofs = (unsigned)wid * 8192 + lz * 16;
        f32x16 give0, give1, keep0, keep1;
        if (ks) { give0 = acc[0][0]; give1 = acc[0][1]; keep0 = acc[1][0]; keep1 = acc[1][1]; }
        else { give0 = acc[1][0]; give1 = acc[1][1]; keep0 = acc[0][0]; keep1 = acc[0][1]; }
#pragma unroll
        for (int g = 0; g < 4; ++g) {
            const f32x4 v0 = {give0[4 * g], give0[4 * g + 1], give0[4 * g + 2], give0[4 * g + 3]}, v1 = {give1[4 * g], give1[4 * g + 1], give1[4 * g + 2], give1[4 * g + 3]};
            __builtin_amdgcn_raw_buffer_store_b128(__builtin_bit_cast(u32x4, v0), rs, (unsigned)(tile * 2 + ks) * 32768 + pofs + g * 1024, 0, 16);
            __builtin_amdgcn_raw_buffer_store_b128(__builtin_bit_cast(u32x4, v1), rs, (unsigned)(tile * 2 + ks) * 32768 + pofs + (4 + g) * 1024, 0, 16);
        }
        asm volatile("s_waitcnt vmcnt(0)" ::: "memory");
        if (lz == 0) __hip_atomic_fetch_add(flags + (tile * 2 + ks) * 32, 1u, __ATOMIC_RELAXED, __HIP_MEMORY_SCOPE_AGENT);
        if (wid == 0) {
            unsigned spins = 0;
            while (__builtin_amdgcn_readfirstlane(__hip_atomic_load(flags + (tile * 2 + (1 - ks)) * 32, __ATOMIC_RELAXED, __HIP_MEMORY_SCOPE_AGENT)) < 4u) { __builtin_amdgcn_s_sleep(1); if (++spins > (1u << 22)) break; }
        }
        asm volatile("s_waitcnt vmcnt(0)\n\ts_barrier" ::: "memory");
        u32x4 pv[8];
#pragma unroll
        for (int i = 0; i < 8; ++i) pv[i] = __builtin_amdgcn_raw_buffer_load_b128(rs, (unsigned)(tile * 2 + (1 - ks)) * 32768 + pofs + i * 1024, 0, 16);
        const int row = pm * 128 + wm * 64 + ks * 32 + rr;
#pragma unroll
        for (int n = 0; n < 2; ++n)
#pragma unroll
            for (int g = 0; g < 4; g += 2) {
                const f32x16 src = n ? keep1 : keep0;
                const f32x4 v0 = (f32x4){src[4 * g], src[4 * g + 1], src[4 * g + 2], src[4 * g + 3]} + __builtin_bit_cast(f32x4, pv[n * 4 + g]);
                const f32x4 v1 = (f32x4){src[4 * g + 4], src[4 * g + 5], src[4 * g + 6], src[4 * g + 7]} + __builtin_bit_cast(f32x4, pv[n * 4 + g + 1]);
                const unsigned ax = cvtpk(v0[0], v0[1]), ay = cvtpk(v0[2], v0[3]), bx = cvtpk(v1[0], v1[1]), by = cvtpk(v1[2], v1[3]);
                auto rx = __builtin_amdgcn_permlane32_swap(ax, bx, false, false); auto ry = __builtin_amdgcn_permlane32_swap(ay, by, false, false);
                const u32x4 w = {rx[0], ry[0], rx[1], ry[1]};
                const int col = pn * 128 + wn * 64 + n * 32 + (g + hh) * 8;
                *(u32x4*)(out + (size_t)row * 1024 + col) = w;
            }
        asm volatile("" :: "v"(dummy));
    }
}

__global__ __launch_bounds__(512, 4) void k23_fused(const u16* __restrict__ QKV, const float* __restrict__ KMAX, const float* __restrict__ G, u16* __restrict__ O,
                                                    const u16* __restrict__ H, const u16* __restrict__ wd, const u16* __restrict__ wo, u16* __restrict__ out, float* __restrict__ P,
                                                    unsigned* __restrict__ flags, unsigned long long* stamps)
{
    extern __shared__ __attribute__((aligned(16))) char lds[];
    const int bid = blockIdx.x;
    unsigned* cnt = flags + 256 * 32;
    if (bid < 256) attn_role(QKV, KMAX, G, O, cnt, lds, bid, stamps);
    else gemm_role(H, O, wd, wo, G, out, P, flags, cnt, lds, bid, stamps);
}

extern "C" void kernel_launch(void* const* d_in, const int* in_sizes, int n_in, void* d_out, int out_size, void* d_ws, size_t ws_size, hipStream_t stream)
{
    const u16* x = (const u16*)d_in[0]; const u16* wqkv = (const u16*)d_in[1]; const u16* wo = (const u16*)d_in[2];
    const u16* wg = (const u16*)d_in[4]; const u16* wu = (const u16*)d_in[5]; const u16* wd = (const u16*)d_in[6]; const u16* gw = (const u16*)d_in[7];
    char* ws = (char*)d_ws;
    u16* QKV = (u16*)ws;
    u16* H = (u16*)(ws + (16u << 20));
    u16* Ob = (u16*)(ws + (32u << 20));
    float* P = (float*)(ws + (40u << 20));
    float* G = (float*)(ws + (48u << 20));
    float* KM = (float*)(ws + (48u << 20) + 65536);
    unsigned* flags = (unsigned*)(ws + (48u << 20) + 131072);
    hipFuncSetAttribute((const void*)k1_gemm, hipFuncAttributeMaxDynamicSharedMemorySize, K1_LDS);
    hipFuncSetAttribute((const void*)k23_fused, hipFuncAttributeMaxDynamicSharedMemorySize, K23_LDS);
    hipLaunchKernelGGL(k1_gemm, dim3(256), dim3(512), K1_LDS, stream, x, wqkv, wg, wu, gw, QKV, H, G, KM, flags, (unsigned long long*)nullptr);
    hipLaunchKernelGGL(k23_fused, dim3(512), dim3(512), K23_LDS, stream, QKV, KM, G, Ob, H, wd, wo, (u16*)d_out, P, flags, (unsigned long long*)nullptr);
}
```

```cpp
#define K1_AUX 2
#include <hip/hip_runtime.h>
#include <stdint.h>
typedef unsigned short u16;
typedef short bf16x8 __attribute__((ext_vector_type(8)));
typedef short s16x4 __attribute__((ext_vector_type(4)));
typedef float f32x4 __attribute__((ext_vector_type(4)));
typedef float f32x16 __attribute__((ext_vector_type(16)));
typedef unsigned u32x4 __attribute__((ext_vector_type(4)));
#define LAS __attribute__((address_space(3)))
typedef LAS const char* lds_cptr;
typedef short v4i16_t __attribute__((ext_vector_type(4)));
#define SBAR() __builtin_amdgcn_sched_barrier(0)

constexpr int S_ = 2048, D_ = 1024, F_ = 3584, NH_ = 32;
constexpr float QSCALE = 0.17677669529663687f * 1.4426950408889634f;

typedef __bf16 bf16x2_t __attribute__((ext_vector_type(2)));
typedef float f32x2_t __attribute__((ext_vector_type(2)));
__device__ __forceinline__ unsigned cvtpk(float lo, float hi) { const f32x2_t v = {lo, hi}; return __builtin_bit_cast(unsigned, __builtin_convertvector(v, bf16x2_t)); }
__device__ __forceinline__ void glds16s(const void* sbase, unsigned voff, unsigned lds_base) {
    asm volatile("s_mov_b32 m0, %2\n\ts_nop 0\n\tglobal_load_lds_dwordx4 %0, %1"
                 :: "v"(voff), "s"(sbase), "s"(lds_base) : "memory");
}
__device__ __forceinline__ s16x4 vtr(lds_cptr p) { return __builtin_bit_cast(s16x4, __builtin_amdgcn_ds_read_tr16_b64_v4i16((LAS v4i16_t*)p)); }
__device__ __forceinline__ bf16x8 cat8(s16x4 a, s16x4 b) { return (bf16x8){a[0], a[1], a[2], a[3], b[0], b[1], b[2], b[3]}; }
__device__ __forceinline__ bf16x8 ldsb128(lds_cptr p) { return *(const LAS bf16x8*)p; }

__device__ __forceinline__ lds_cptr LP(unsigned a) { return (lds_cptr)(uintptr_t)a; }
template <int X> __device__ __forceinline__ float swz_xor(float v) { return __int_as_float(__builtin_amdgcn_ds_swizzle(__float_as_int(v), (X << 10) | 0x1f)); }
__device__ __forceinline__ float add32(float v) { auto r = __builtin_amdgcn_permlane32_swap(__float_as_uint(v), __float_as_uint(v), false, false); return __uint_as_float(r[0]) + __uint_as_float(r[1]); }
__device__ __forceinline__ float max32(float v) { auto r = __builtin_amdgcn_permlane32_swap(__float_as_uint(v), __float_as_uint(v), false, false); return fmaxf(__uint_as_float(r[0]), __uint_as_float(r[1])); }
constexpr int K1_STAGE = 73728, K1_LDS = 2 * K1_STAGE;

__device__ __forceinline__ float gelu_mul(float g, float u) {
    const float t = g * g;
    const float p = __builtin_fmaf(t, -0.1029432397f, -2.302208198f);
    const float e = __builtin_amdgcn_exp2f(g * p);
    const float r = __builtin_amdgcn_rcpf(1.0f + e);
    return g * u * r;
}

__global__ __launch_bounds__(512, 2) void k1_gemm(const u16* __restrict__ x, const u16* __restrict__ wqkv, const u16* __restrict__ wg, const u16* __restrict__ wu,
                                                  const u16* __restrict__ gw, u16* __restrict__ QKV, u16* __restrict__ H, float* __restrict__ G, float* __restrict__ KMAX, unsigned* __restrict__ flags, const u16* __restrict__ wd, const u16* __restrict__ wo, unsigned long long* stamps)
{
    extern __shared__ __attribute__((aligned(16))) char lds[];
    const int tid = threadIdx.x, lane = tid & 63;
    const int wid = __builtin_amdgcn_readfirstlane(tid >> 6);
    const int wm = wid & 3, wn = wid >> 2;
    const int fr = lane & 15, fq = lane >> 4;
    const int bid = blockIdx.x, xcd = bid & 7, idx = bid >> 3, pm = idx & 7, pn = xcd * 4 + (idx >> 3);
    const bool typeB = pn >= 24;
    const unsigned lds0 = (unsigned)(uintptr_t)lds;
    unsigned long long ts0=0ull, tr0=0ull;

    if (bid == 0) { for (int i = tid; i < 264 * 32; i += 512) flags[i] = 0u; }
    const char* cb0; const char* cb1; const char* cb2; const char* cb3; const char* cb4;
    unsigned ldmid;
    if (!typeB) { cb0 = (const char*)(wg + 128 * pn); cb1 = (const char*)(wu + 128 * pn); cb2 = cb0 + 128; cb3 = cb1 + 128; cb4 = (const char*)(wqkv + 64 * pn); ldmid = F_ * 2; }
    else { const int i = pn - 24; cb0 = (const char*)(wg + 3072 + 64 * i); cb1 = (const char*)(wu + 3072 + 64 * i); cb2 = (const char*)(wqkv + 64 * (24 + 3 * i)); cb3 = cb2 + 128; cb4 = cb2 + 256; ldmid = 3072 * 2; }
    cb0 += (size_t)(8 * wid) * (F_ * 2); cb1 += (size_t)(8 * wid) * (F_ * 2); cb2 += (size_t)(8 * wid) * ldmid; cb3 += (size_t)(8 * wid) * ldmid; cb4 += (size_t)(8 * wid) * (3072 * 2);
    const char* ca = (const char*)x + (size_t)(256 * pm + 8 * wid) * 2048;
    const int sr = lane >> 3, spos = lane & 7;
    const unsigned fB = ((sr >> 1) & 1) | ((wid & 1) << 1);
    const unsigned colB = (((spos >> 1) ^ fB) * 32) + (spos & 1) * 16;
    const unsigned voffG = sr * (F_ * 2) + colB, voffM = sr * ldmid + colB, voffQ = sr * (3072 * 2) + colB;
    const unsigned voffA = sr * 2048 + ((spos ^ (4 * (wid & 1) + (sr >> 1))) * 16);
    const unsigned ldsw = lds0 + wid * 1024;
    float gsig;

#define K1_PIECE(i, sb) do { \
        if ((i) < 4) glds16s(ca + (size_t)(i) * 131072, voffA, (sb) + (i) * 8192); \
        else if ((i) == 4) glds16s(cb0, voffG, (sb) + 32768); \
        else if ((i) == 5) glds16s(cb2, voffM, (sb) + 32768 + 16384); \
        else if ((i) == 6) glds16s(cb1, voffG, (sb) + 32768 + 8192); \
        else if ((i) == 7) glds16s(cb3, voffM, (sb) + 32768 + 24576); \
        else glds16s(cb4, voffQ, (sb) + 32768 + 32768); } while (0)
#define K1_ADV1() do { ca += 128; cb0 += 64 * F_ * 2; cb2 += (size_t)64 * ldmid; } while (0)
#define K1_ADV2() do { cb1 += 64 * F_ * 2; cb3 += (size_t)64 * ldmid; cb4 += 64 * 3072 * 2; } while (0)

    const int grow = 256 * pm + 8 * pn + wid;
    u32x4 gx0, gx1, gg0, gg1;
    {
        const char* xr = (const char*)(x + (size_t)grow * D_);
        const unsigned go = lane * 32;
        asm volatile("global_load_dwordx4 %0, %4, %5\n\tglobal_load_dwordx4 %1, %4, %5 offset:16\n\tglobal_load_dwordx4 %2, %4, %6\n\tglobal_load_dwordx4 %3, %4, %6 offset:16"
                     : "=&v"(gx0), "=&v"(gx1), "=&v"(gg0), "=&v"(gg1) : "v"(go), "s"(xr), "s"(gw) : "memory");
    }
#pragma unroll
    for (int i = 0; i < 9; ++i) K1_PIECE(i, ldsw);
    K1_ADV1(); K1_ADV2();
#pragma unroll
    for (int i = 0; i < 7; ++i) K1_PIECE(i, ldsw + K1_STAGE);
    K1_ADV1();
    unsigned long long tsa=0ull;
    asm volatile("s_waitcnt vmcnt(16)" : "+v"(gx0), "+v"(gx1), "+v"(gg0), "+v"(gg1) :: "memory");
    {
        float s = 0.f;
#pragma unroll
        for (int j = 0; j < 4; ++j) {
            s += __uint_as_float(gx0[j] << 16) * __uint_as_float(gg0[j] << 16) + __uint_as_float(gx0[j] & 0xffff0000u) * __uint_as_float(gg0[j] & 0xffff0000u);
            s += __uint_as_float(gx1[j] << 16) * __uint_as_float(gg1[j] << 16) + __uint_as_float(gx1[j] & 0xffff0000u) * __uint_as_float(gg1[j] & 0xffff0000u);
        }
        s += swz_xor<1>(s); s += swz_xor<2>(s); s += swz_xor<4>(s); s += swz_xor<8>(s); s += swz_xor<16>(s); s = add32(s);
        gsig = 1.0f / (1.0f + __expf(-s));
    }

    unsigned long long tsb=0ull;
    unsigned aA0 = lds0 + (64 * wm + fr) * 128 + (((0 + fq) ^ (fr >> 1)) * 16);
    unsigned aA1 = lds0 + (64 * wm + fr) * 128 + (((4 + fq) ^ (fr >> 1)) * 16);
    const int tq = (lane & 15) >> 2, tp = lane & 3;
    const unsigned fR = ((tq >> 1) & 1) | ((fq & 1) << 1);
    const unsigned bbase = lds0 + 32768 + (8 * fq + tq) * 128 + 8 * tp;
    unsigned aB0 = bbase + 2 * wn * 8192 + ((0 ^ fR) << 5);
    unsigned aB1 = bbase + 2 * wn * 8192 + ((1 ^ fR) << 5);
    unsigned aB2 = bbase + 2 * wn * 8192 + ((2 ^ fR) << 5);
    unsigned aB3 = bbase + 2 * wn * 8192 + ((3 ^ fR) << 5);
    unsigned aB4a = bbase + 4 * 8192 + (((2 * wn) ^ fR) << 5);
    unsigned aB4b = bbase + 4 * 8192 + (((2 * wn + 1) ^ fR) << 5);

    f32x4 acc[4][10];
#pragma unroll
    for (int m = 0; m < 4; ++m)
#pragma unroll
        for (int s = 0; s < 10; ++s) acc[m][s] = (f32x4){0.f, 0.f, 0.f, 0.f};
    bf16x8 fa0[4], fa1[4];
    s16x4 bx[4], by[4];

#define MF(fb, h, fa, m, ti) acc[m][(ti) + (h)] = __builtin_amdgcn_mfma_f32_16x16x32_bf16(cat8(fb[2 * (h)], fb[2 * (h) + 1]), fa[m], acc[m][(ti) + (h)], 0, 0, 0)
#define RB(dst, j, aa, ab, off) dst[j] = vtr(LP(((j) < 2) ? (aa) : (ab)) + (off) + ((j) & 1) * 512)
#define RA(dst, m, a) dst[m] = ldsb128(LP(a) + (m) * 2048)
#define SUBP(cur, fa, ti, nxt, aa, ab, off, AR, GL) do { \
        MF(cur, 0, fa, 0, ti); SBAR(); RB(nxt, 0, aa, ab, off); SBAR(); \
        MF(cur, 1, fa, 0, ti); SBAR(); RB(nxt, 1, aa, ab, off); SBAR(); \
        MF(cur, 0, fa, 1, ti); SBAR(); RB(nxt, 2, aa, ab, off); SBAR(); \
        MF(cur, 1, fa, 1, ti); SBAR(); RB(nxt, 3, aa, ab, off); SBAR(); \
        MF(cur, 0, fa, 2, ti); SBAR(); AR(0); GL; SBAR(); \
        MF(cur, 1, fa, 2, ti); SBAR(); AR(1); SBAR(); \
        MF(cur, 0, fa, 3, ti); SBAR(); AR(2); SBAR(); \
        MF(cur, 1, fa, 3, ti); SBAR(); AR(3); SBAR(); } while (0)
#define NOA(m) do { } while (0)
#define RA0(m) RA(fa0, m, aA0)
#define RA1(m) RA(fa1, m, aA1)
#define NOG do { } while (0)
#define WBAR(n) asm volatile("s_waitcnt vmcnt(" #n ") lgkmcnt(0)\n\ts_barrier" ::: "memory")

    unsigned warm0 = 0;
    const int wgi = bid * 8 + wid;
    const char* wp = wgi < 1275 ? (const char*)wd + min(wgi * 5760, 7340032 - 5760) : (const char*)wo + min((wgi - 1275) * 5760, 2097152 - 5760);
    const unsigned wvo = (lane % 6) * 64;
    asm volatile("global_load_dword %0, %1, %2" : "+v"(warm0) : "v"(wvo), "s"(wp) : "memory"); wp += 384;
    K1_PIECE(6, ldsw + K1_STAGE);
    asm volatile("s_waitcnt vmcnt(11)\n\ts_barrier" ::: "memory");
    if (lane == 0) G[grow] = gsig;
#pragma unroll
    for (int j = 0; j < 4; ++j) RB(bx, j, aB0, aB1, 0);
#pragma unroll
    for (int m = 0; m < 4; ++m) RA0(m);
    int delta = K1_STAGE;
    unsigned long long ts1=0ull; tsa=0ull;

#define WBARM() WBAR(10)
#define VADD(a) asm volatile("v_add_u32 %0, %1, %0" : "+v"(a) : "s"(delta))
#define STEP(MAIN) do { \
        SUBP(bx, fa0, 0, by, aB0, aB1, 4096, RA1, if (MAIN || p2) K1_PIECE(7, ns)); \
        SUBP(by, fa1, 0, bx, aB2, aB3, 0, NOA, if (MAIN || p2) { K1_PIECE(8, ns); K1_ADV2(); }); \
        SUBP(bx, fa0, 2, by, aB2, aB3, 4096, NOA, NOG); \
        if (MAIN) WBARM(); else if (p2) WBAR(9); else WBAR(0); \
        SUBP(by, fa1, 2, bx, aB0, aB1, 8192, NOA, if (MAIN) K1_PIECE(0, cs)); \
        SUBP(bx, fa0, 4, by, aB0, aB1, 8192 + 4096, NOA, if (MAIN) K1_PIECE(1, cs)); \
        SUBP(by, fa1, 4, bx, aB2, aB3, 8192, NOA, if (MAIN) K1_PIECE(2, cs)); \
        SUBP(bx, fa0, 6, by, aB2, aB3, 8192 + 4096, NOA, if (MAIN) K1_PIECE(3, cs)); \
        SUBP(by, fa1, 6, bx, aB4a, aB4b, 0, NOA, if (MAIN) K1_PIECE(4, cs)); \
        SUBP(bx, fa0, 8, by, aB4a, aB4b, 4096, NOA, if (MAIN) { K1_PIECE(5, cs); K1_ADV1(); }); \
        VADD(aA0); VADD(aA1); VADD(aB0); VADD(aB1); VADD(aB2); VADD(aB3); \
        if (MAIN) WBARM(); else WBAR(3); \
        SUBP(by, fa1, 8, bx, aB0, aB1, 0, RA0, if (MAIN) K1_PIECE(6, cs)); \
        VADD(aB4a); VADD(aB4b); delta = -delta; { const unsigned tmp = cs; cs = ns; ns = tmp; } \
    } while (0)
    unsigned cs = ldsw, ns = ldsw + K1_STAGE;
#pragma nounroll
    for (int tt = 0; tt < 14; ++tt) {
        const bool p2 = true; STEP(true); asm volatile("" : "+s"(cs), "+s"(ns), "+s"(delta));
        asm volatile("global_load_dword %0, %1, %2" : "+v"(warm0) : "v"(wvo), "s"(wp) : "memory"); wp += 384;
    }
#pragma nounroll
    for (int tt = 0; tt < 2; ++tt) { int t = tt; asm volatile("" : "+s"(t)); const bool p2 = t == 0; STEP(false); asm volatile("" : "+s"(cs), "+s"(ns), "+s"(delta)); }
    unsigned long long ts2=0ull; tsb=0ull;

    const int row0 = 256 * pm + 64 * wm;
    const __amdgpu_buffer_rsrc_t rsH = __builtin_amdgcn_make_buffer_rsrc(H, 0, 2048 * 3584 * 2, 0x00020000), rsQ = __builtin_amdgcn_make_buffer_rsrc(QKV, 0, 96 * 2048 * 64, 0x00020000);
#define ST16(base, rs, off, w) __builtin_amdgcn_raw_buffer_store_b128(w, rs, off, 0, K1_AUX)
    const unsigned swz = ((fq ^ ((-(fr >> 2)) & 3)) * 16) + fr * 64;
    const bool gu = !typeB || wn == 0;
    if (gu) {
        const int hc0 = typeB ? (3072 + 64 * (pn - 24)) : (128 * pn + 64 * wn);
#pragma unroll
        for (int m = 0; m < 4; ++m) {
            const int mt = (row0 >> 4) + m;
#pragma unroll
            for (int i = 0; i < 2; ++i) {
                u32x4 w;
                { const f32x4 g = acc[m][2 * i], u = acc[m][4 + 2 * i];
                  w[0] = cvtpk(gelu_mul(g[0], u[0]), gelu_mul(g[1], u[1])); w[1] = cvtpk(gelu_mul(g[2], u[2]), gelu_mul(g[3], u[3])); }
                { const f32x4 g = acc[m][2 * i + 1], u = acc[m][4 + 2 * i + 1];
                  w[2] = cvtpk(gelu_mul(g[0], u[0]), gelu_mul(g[1], u[1])); w[3] = cvtpk(gelu_mul(g[2], u[2]), gelu_mul(g[3], u[3])); }
                ST16(H, rsH, ((unsigned)mt * 112 + (hc0 >> 5) + i) * 1024 + swz, w);
            }
        }
    }
    auto store_head = [&](int hb, const f32x4 (&ta)[4], const f32x4 (&tb)[4]) {
        const int which = hb >> 5;
        const float sc = which == 0 ? QSCALE : 1.0f;
        float kn = 0.f;
#pragma unroll
        for (int m = 0; m < 4; ++m) {
            const f32x4 a = ta[m] * sc, b = tb[m] * sc;
            u32x4 w; w[0] = cvtpk(a[0], a[1]); w[1] = cvtpk(a[2], a[3]); w[2] = cvtpk(b[0], b[1]); w[3] = cvtpk(b[2], b[3]);
            ST16(QKV, rsQ, ((unsigned)hb * 2048 + row0 + 16 * m + fr) * 64 + fq * 16, w);
            if (which == 1) {
                float n2 = a[0] * a[0] + a[1] * a[1] + a[2] * a[2] + a[3] * a[3] + b[0] * b[0] + b[1] * b[1] + b[2] * b[2] + b[3] * b[3];
                n2 += swz_xor<16>(n2); n2 = add32(n2);
                kn = fmaxf(kn, n2);
            }
        }
        if (which == 1) {
            kn = fmaxf(kn, swz_xor<8>(kn)); kn = fmaxf(kn, swz_xor<4>(kn)); kn = fmaxf(kn, swz_xor<2>(kn)); kn = fmaxf(kn, swz_xor<1>(kn));
            if (lane == 0) KMAX[(hb & 31) * 32 + pm * 4 + wm] = kn;
        }
    };
    {
        const int qc4 = typeB ? (24 + 3 * (pn - 24) + 2) : pn;
        const f32x4 ta[4] = {acc[0][8], acc[1][8], acc[2][8], acc[3][8]};
        const f32x4 tb[4] = {acc[0][9], acc[1][9], acc[2][9], acc[3][9]};
        store_head(2 * qc4 + wn, ta, tb);
    }
    if (!gu) {
        const int qc2 = 24 + 3 * (pn - 24);
#pragma unroll
        for (int c = 0; c < 2; ++c)
#pragma unroll
            for (int hh = 0; hh < 2; ++hh) {
                const f32x4 ta[4] = {acc[0][4 * c + 2 * hh], acc[1][4 * c + 2 * hh], acc[2][4 * c + 2 * hh], acc[3][4 * c + 2 * hh]};
                const f32x4 tb[4] = {acc[0][4 * c + 2 * hh + 1], acc[1][4 * c + 2 * hh + 1], acc[2][4 * c + 2 * hh + 1], acc[3][4 * c + 2 * hh + 1]};
                store_head(2 * (qc2 + c) + hh, ta, tb);
            }
    }
    asm volatile("" :: "v"(warm0));
}

#define MFMA32(a, b, c) __builtin_amdgcn_mfma_f32_32x32x16_bf16(a, b, c, 0, 0, 0)
#define MFMA16(a, b, c) __builtin_amdgcn_mfma_f32_16x16x32_bf16(a, b, c, 0, 0, 0)
constexpr int K2_SLOT = 16384, K2_NS = 4, K2_LDS = K2_SLOT * K2_NS;

__device__ __forceinline__ bf16x8 pack8(const f32x16& p, int b) {
    u32x4 w; w[0] = cvtpk(p[b], p[b + 1]); w[1] = cvtpk(p[b + 2], p[b + 3]); w[2] = cvtpk(p[b + 4], p[b + 5]); w[3] = cvtpk(p[b + 6], p[b + 7]);
    return __builtin_bit_cast(bf16x8, w);
}

#define K2_ISSUE(st) do { glds16s(Kh + (size_t)(st) * 8192, voffK, ldsw + ((st) & 3) * K2_SLOT); glds16s(Vh + (size_t)(st) * 8192, voffV, ldsw + ((st) & 3) * K2_SLOT + 8192); } while (0)
#define RDK(d0, d1, slot, i) do { d0 = ldsb128(LP(aK0 + (slot)) + (i) * 2048); d1 = ldsb128(LP(aK1 + (slot)) + (i) * 2048); } while (0)
#define QK(S, k0, k1) do { S = MFMA32(k0, qf0, negm); S = MFMA32(k1, qf1, S); } while (0)
#define PIN(x) asm volatile("" : "+v"(x))
#define PKA(a, S, i) a += (f32x2_t){S[i], S[(i) + 1]}
#define SUM0 do { PKA(ls0, SCUR, 0); PKA(ls1, SCUR, 2); PKA(ls0, SCUR, 4); PKA(ls1, SCUR, 6); PIN(ls0); PIN(ls1); } while (0)
#define SUM1 do { PKA(ls0, SCUR, 8); PKA(ls1, SCUR, 10); PKA(ls0, SCUR, 12); PKA(ls1, SCUR, 14); PIN(ls0); PIN(ls1); } while (0)
#define SUM0 lsum = MFMA16(sel, p0, lsum)
#define SUM1 lsum2 = MFMA16(sel, p1, lsum2)
#define EX4(S, b) do { S[b] = __builtin_amdgcn_exp2f(S[b]); S[b + 1] = __builtin_amdgcn_exp2f(S[b + 1]); S[b + 2] = __builtin_amdgcn_exp2f(S[b + 2]); S[b + 3] = __builtin_amdgcn_exp2f(S[b + 3]); } while (0)
#define RDV(d, slot, i) do { d[0] = vtr(LP(aV + (slot)) + (i) * 2048); d[1] = vtr(LP(aV + (slot)) + (i) * 2048 + 512); d[2] = vtr(LP(aV + (slot)) + (i) * 2048 + 1024); d[3] = vtr(LP(aV + (slot)) + (i) * 2048 + 1536); } while (0)
#define ITER(SC, SN, k0, k1, n0, n1, vc, vn, kslot, ki, vslot, vi) do { \
        f32x16& SCUR = SC; (void)SCUR; \
        SN = MFMA32(k0, qf0, negm); SBAR(); \
        EX4(SC, 0); PIN(SC); SBAR(); \
        SN = MFMA32(k1, qf1, SN); SBAR(); \
        RDK(n0, n1, kslot, ki); SBAR(); \
        EX4(SC, 4); const bf16x8 p0 = pack8(SC, 0); SBAR(); \
        o = MFMA32(cat8(vc[0], vc[1]), p0, o); SBAR(); \
        EX4(SC, 8); PIN(SC); SBAR(); \
        SUM0; SBAR(); \
        EX4(SC, 12); const bf16x8 p1 = pack8(SC, 8); SBAR(); \
        o = MFMA32(cat8(vc[2], vc[3]), p1, o); SBAR(); \
        RDV(vn, vslot, vi); SBAR(); \
        SUM1; SBAR(); \
    } while (0)
#define WB2(n) asm volatile("s_waitcnt vmcnt(" #n ") lgkmcnt(0)\n\ts_barrier" ::: "memory")
constexpr int K23_LDS = 81920;
#define GEMM_PRIO 0
#define ATT_PRIO 0

__device__ __forceinline__ void attn_role(const u16* __restrict__ QKV, const float* __restrict__ KMAX, const float* __restrict__ G, u16* __restrict__ O, unsigned* __restrict__ cnt, char* lds, int bid, unsigned long long* stamps)
{
    const int tid = threadIdx.x, lane = tid & 63;
    const int wid = __builtin_amdgcn_readfirstlane(tid >> 6);
    const int r32 = lane & 31, hi = lane >> 5;
    const int xcd = bid & 7, idx = bid >> 3, head = 4 * xcd + (idx >> 3), qb = idx & 7;
    const unsigned lds0 = (unsigned)(uintptr_t)lds;
    unsigned long long ts0 = 0ull, tr0 = 0ull;
    if (ATT_PRIO) __builtin_amdgcn_s_setprio(ATT_PRIO);
    const char* Qh = (const char*)QKV + (size_t)head * 131072;
    const char* Kh = (const char*)QKV + (size_t)(32 + head) * 131072 + wid * 1024;
    const char* Vh = (const char*)QKV + (size_t)(64 + head) * 131072 + wid * 1024;
    const unsigned voffK = (lane >> 2) * 64 + (((lane & 3) ^ ((lane >> 4) & 3)) * 16), voffV = lane * 16;
    const unsigned ldsw = lds0 + wid * 1024;
    K2_ISSUE(0); K2_ISSUE(1);
    const int q0 = qb * 256 + wid * 32;
    const bf16x8 qf0 = *(const bf16x8*)(Qh + (size_t)(q0 + r32) * 64 + hi * 16);
    const bf16x8 qf1 = *(const bf16x8*)(Qh + (size_t)(q0 + r32) * 64 + 32 + hi * 16);
    const float kmx = KMAX[head * 32 + r32];
    float qn2 = 0.f;
#pragma unroll
    for (int j = 0; j < 8; ++j) { const float a = __uint_as_float((unsigned)(u16)qf0[j] << 16), b = __uint_as_float((unsigned)(u16)qf1[j] << 16); qn2 += a * a + b * b; }
    qn2 = add32(qn2);
    float km2 = kmx; km2 = fmaxf(km2, swz_xor<1>(km2)); km2 = fmaxf(km2, swz_xor<2>(km2)); km2 = fmaxf(km2, swz_xor<4>(km2)); km2 = fmaxf(km2, swz_xor<8>(km2)); km2 = fmaxf(km2, swz_xor<16>(km2));
    const float mb = sqrtf(qn2 * km2) * 1.01f;
    f32x16 negm;
#pragma unroll
    for (int r = 0; r < 16; ++r) negm[r] = -mb;
    asm volatile("" : "+v"(negm));
    bf16x8 sel;
    { const short one = ((lane & 15) == ((lane >> 4) & 1)) ? (short)0x3F80 : (short)0;
#pragma unroll
      for (int j = 0; j < 8; ++j) sel[j] = one; }
    const unsigned aK0 = lds0 + r32 * 64 + (((0 + hi) ^ ((r32 >> 2) & 3)) * 16);
    const unsigned aK1 = lds0 + r32 * 64 + (((2 + hi) ^ ((r32 >> 2) & 3)) * 16);
    const unsigned aV = lds0 + 8192 + (4 * hi + ((lane & 15) >> 2)) * 64 + ((lane >> 4) & 1) * 32 + (lane & 3) * 8;
    f32x16 o = {}; f32x4 lsum = {0.f, 0.f, 0.f, 0.f}, lsum2 = {0.f, 0.f, 0.f, 0.f}; f32x2_t ls0 = {0.f, 0.f}, ls1 = {0.f, 0.f};
    f32x16 sA, sB;
    bf16x8 kfa0, kfa1, kfb0, kfb1;
    s16x4 va[4], vb[4];
    WB2(2);
    K2_ISSUE(2);
    RDK(kfa0, kfa1, 0, 0); RDK(kfb0, kfb1, 0, 1); RDV(va, 0, 0);
    QK(sA, kfa0, kfa1);
    unsigned long long ts1 = 0ull;
#pragma nounroll
    for (int st = 0; st < 16; ++st) {
        int stv = st; asm volatile("" : "+s"(stv));
        const unsigned cur = (stv & 3) * K2_SLOT, nxt = ((stv + 1) & 3) * K2_SLOT;
        ITER(sA, sB, kfb0, kfb1, kfa0, kfa1, va, vb, cur, 2, cur, 1);
        ITER(sB, sA, kfa0, kfa1, kfb0, kfb1, vb, va, cur, 3, cur, 2);
        if (stv <= 12) { WB2(2); K2_ISSUE(stv + 3); } else if (stv == 13) WB2(2); else WB2(0);
        ITER(sA, sB, kfb0, kfb1, kfa0, kfa1, va, vb, nxt, 0, cur, 3);
        ITER(sB, sA, kfa0, kfa1, kfb0, kfb1, vb, va, nxt, 1, nxt, 0);
    }
    lsum += lsum2;
    unsigned long long ts2 = 0ull;
    int lz = threadIdx.x & 63; asm volatile("" : "+v"(lz));
    const int rr = lz & 31, hh = lz >> 5;
    const float gq = G[q0 + rr];
    const float l0 = __int_as_float(__builtin_amdgcn_ds_bpermute((rr & 15) << 2, __float_as_int(lsum[0]))), l1 = __int_as_float(__builtin_amdgcn_ds_bpermute((rr & 15) << 2, __float_as_int(lsum[1])));
    const float l = (rr & 16) ? l1 : l0;
    const float sc = gq / l;
    const int row = q0 + rr, fr = row & 15, mt = row >> 4;
    const __amdgpu_buffer_rsrc_t rs = __builtin_amdgcn_make_buffer_rsrc(O, 0, 2048 * 1024 * 2, 0x00020000);
    const unsigned ob = (unsigned)(mt * 32 + head) * 1024 + fr * 64;
    const int gsw = (-(fr >> 2)) & 3;
#pragma unroll
    for (int c = 0; c < 4; c += 2) {
        unsigned ax = cvtpk(o[4 * c] * sc, o[4 * c + 1] * sc), ay = cvtpk(o[4 * c + 2] * sc, o[4 * c + 3] * sc);
        unsigned bx = cvtpk(o[4 * c + 4] * sc, o[4 * c + 5] * sc), by = cvtpk(o[4 * c + 6] * sc, o[4 * c + 7] * sc);
        auto rx = __builtin_amdgcn_permlane32_swap(ax, bx, false, false); auto ry = __builtin_amdgcn_permlane32_swap(ay, by, false, false);
        const u32x4 w = {rx[0], ry[0], rx[1], ry[1]};
        __builtin_amdgcn_raw_buffer_store_b128(w, rs, ob + (((c + hh) ^ gsw) * 16), 0, 2);
    }
}

#define G_RD(x, w, so, s) do { \
        x[0] = ldsb128(LP((aX0 + (so)) ^ ((s) * 32))); w[0] = vtr(LP(aW0 + (so)) + (s) * 2048); w[1] = vtr(LP(aW0 + (so)) + (s) * 2048 + 512); \
        x[1] = ldsb128(LP((aX0 + (so)) ^ ((s) * 32)) + 2048); w[2] = vtr(LP((aW0 + (so)) ^ 64) + (s) * 2048); w[3] = vtr(LP((aW0 + (so)) ^ 64) + (s) * 2048 + 512); } while (0)
#define G_MM(x, w) do { \
        acc[0][0] = MFMA32(cat8(w[0], w[1]), x[0], acc[0][0]); acc[1][0] = MFMA32(cat8(w[0], w[1]), x[1], acc[1][0]); \
        acc[0][1] = MFMA32(cat8(w[2], w[3]), x[0], acc[0][1]); acc[1][1] = MFMA32(cat8(w[2], w[3]), x[1], acc[1][1]); } while (0)
#define DBGADV pa += 1024; pb += 32 * 2048;
#define PFD 6
__device__ __forceinline__ void gemm_role(const u16* __restrict__ H, const u16* __restrict__ wd, float* __restrict__ P, char* lds, int bid, unsigned long long* stamps)
{
    const int tid = threadIdx.x, lane = tid & 63;
    const int wid = __builtin_amdgcn_readfirstlane(tid >> 6);
    const int wm = wid & 1, wn = (wid >> 1) & 1, wk = wid >> 2;
    const int jb = bid - 256, xcd = jb & 7, idx = jb >> 3;
    const int ks = xcd & 1, pnl = idx >> 3, pml = idx & 7, pn = 4 * ((xcd >> 1) & 1) + pnl, pm = 8 * (xcd >> 2) + pml;
    const unsigned lds0 = (unsigned)(uintptr_t)lds;
    unsigned long long ts0 = 0ull, tr0 = 0ull, tA0, tA1;
    if (GEMM_PRIO) __builtin_amdgcn_s_setprio(GEMM_PRIO);
#define VW(n) asm volatile("s_waitcnt vmcnt(" #n ") lgkmcnt(0)\n\ts_barrier" ::: "memory")
    const int sr = lane >> 3, spos = lane & 7;
    const unsigned voffB = ((sr & 3) + 16 * (sr >> 2)) * 2048 + ((spos ^ (((sr >> 1) & 1) << 2)) * 16);
    const unsigned voffA = lane * 16;
    const unsigned ldsA = lds0 + wid * 1024, ldsB = lds0 + 8192 + wid * 1024;
    const char* pa = (const char*)H + ((size_t)(pm * 8 + wid) * 112 + 56 * ks) * 1024;
    const char* pb = (const char*)wd + (size_t)(56 * ks * 32 + 4 * (wid & 3)) * 2048 + pn * 256 + (wid >> 2) * 128;
#define K4_ISSUE() do { const unsigned so_ = slot_i * 16384; glds16s(pa, voffA, ldsA + so_); glds16s(pb, voffB, ldsB + so_); pa += 1024; pb += 32 * 2048; slot_i = (slot_i == 4) ? 0 : slot_i + 1; } while (0)
    const int r32 = lane & 31, hi = lane >> 5, fr = lane & 15;
    const unsigned aX0 = lds0 + (wm * 4 + (r32 >> 4)) * 1024 + fr * 64 + ((hi ^ ((-(fr >> 2)) & 3)) * 16);
    const int tq = (lane & 15) >> 2, tp = lane & 3;
    const unsigned aW0 = lds0 + 8192 + wn * 4096 + (8 * hi + tq) * 128 + (((tq >> 1) & 1) << 6) + ((lane >> 4) & 1) * 32 + tp * 8;
    f32x16 acc[2][2];
#pragma unroll
    for (int m = 0; m < 2; ++m)
#pragma unroll
        for (int n = 0; n < 2; ++n) acc[m][n] = (f32x16){};
    bf16x8 xp[2] = {}, xq[2] = {}; s16x4 wp[4] = {}, wq[4] = {};
    int slot_i = 0;
#pragma unroll
    for (int i = 0; i < 5; ++i) K4_ISSUE();
    int rs_i = wk;
    VW(6);
    tA0 = 0ull;
    G_RD(xp, wp, rs_i * 16384, 0);
#define G_STEP(W, I1, I2) do { \
        const unsigned so = rs_i * 16384; \
        rs_i += 2; rs_i = (rs_i >= 5) ? rs_i - 5 : rs_i; \
        const unsigned sn = rs_i * 16384; \
        G_RD(xq, wq, so, 1); SBAR(); G_MM(xp, wp); SBAR(); \
        VW(W); \
        G_RD(xp, wp, sn, 0); SBAR(); if (I1) K4_ISSUE(); if (I2) K4_ISSUE(); SBAR(); G_MM(xq, wq); SBAR(); } while (0)
#pragma nounroll
    for (int t = 0; t < 25; ++t) { asm volatile("" : "+s"(rs_i), "+s"(slot_i)); G_STEP(2, true, true); }
    G_STEP(2, true, false);
    G_STEP(0, false, false);
    G_STEP(0, false, false);
    asm volatile("s_waitcnt lgkmcnt(0)\n\ts_barrier" ::: "memory");
    tA1 = 0ull;
    {
        int lz = threadIdx.x & 63; asm volatile("" : "+v"(lz));
        const int rr = lz & 31, hh = lz >> 5;
        f32x16 give0, give1, keep0, keep1;
        if (wk) { give0 = acc[0][0]; give1 = acc[0][1]; keep0 = acc[1][0]; keep1 = acc[1][1]; }
        else { give0 = acc[1][0]; give1 = acc[1][1]; keep0 = acc[0][0]; keep1 = acc[0][1]; }
        LAS f32x4* xs = (LAS f32x4*)(lds + ((wid & 3) * 2 + wk) * 8192) + lz;
#pragma unroll
        for (int g = 0; g < 4; ++g) {
            xs[g * 64] = (f32x4){give0[4 * g], give0[4 * g + 1], give0[4 * g + 2], give0[4 * g + 3]};
            xs[(4 + g) * 64] = (f32x4){give1[4 * g], give1[4 * g + 1], give1[4 * g + 2], give1[4 * g + 3]};
        }
        asm volatile("s_waitcnt lgkmcnt(0)\n\ts_barrier" ::: "memory");
        const LAS f32x4* xr = (const LAS f32x4*)(lds + ((wid & 3) * 2 + (1 - wk)) * 8192) + lz;
        u32x4* pd = (u32x4*)P + ((size_t)((ks * 16 + pm) * 8 + pn) * 8 + (wm * 2 + wk) * 2 + wn) * 256 + lz;
#pragma unroll
        for (int gp = 0; gp < 2; ++gp) {
            const int g = 2 * gp;
            const f32x4 a0 = (f32x4){keep0[4 * g], keep0[4 * g + 1], keep0[4 * g + 2], keep0[4 * g + 3]} + xr[g * 64];
            const f32x4 a1 = (f32x4){keep0[4 * g + 4], keep0[4 * g + 5], keep0[4 * g + 6], keep0[4 * g + 7]} + xr[(g + 1) * 64];
            const f32x4 b0 = (f32x4){keep1[4 * g], keep1[4 * g + 1], keep1[4 * g + 2], keep1[4 * g + 3]} + xr[(4 + g) * 64];
            const f32x4 b1 = (f32x4){keep1[4 * g + 4], keep1[4 * g + 5], keep1[4 * g + 6], keep1[4 * g + 7]} + xr[(4 + g + 1) * 64];
            const u32x4 wa = {cvtpk(a0[0], a0[1]), cvtpk(a0[2], a0[3]), cvtpk(a1[0], a1[1]), cvtpk(a1[2], a1[3])};
            const u32x4 wb = {cvtpk(b0[0], b0[1]), cvtpk(b0[2], b0[3]), cvtpk(b1[0], b1[1]), cvtpk(b1[2], b1[3])};
            __builtin_nontemporal_store(wa, pd + gp * 64);
            __builtin_nontemporal_store(wb, pd + (2 + gp) * 64);
        }
        (void)rr; (void)hh;
    }
}

#define UNP(a, b, i) ((f32x4){__uint_as_float(a[i] << 16) + __uint_as_float(b[i] << 16), __uint_as_float(a[i] & 0xffff0000u) + __uint_as_float(b[i] & 0xffff0000u), \
                              __uint_as_float(a[(i) + 1] << 16) + __uint_as_float(b[(i) + 1] << 16), __uint_as_float(a[(i) + 1] & 0xffff0000u) + __uint_as_float(b[(i) + 1] & 0xffff0000u)})
constexpr int K4_LDS = 147456;
__global__ __launch_bounds__(512, 2) void k4_out(const u16* __restrict__ Ob, const u16* __restrict__ wo, const float* __restrict__ P, const float* __restrict__ G, u16* __restrict__ out, unsigned long long* stamps)
{
    extern __shared__ __attribute__((aligned(16))) char lds[];
    const int tid = threadIdx.x, lane = tid & 63;
    const int wid = __builtin_amdgcn_readfirstlane(tid >> 6);
    const int wm = wid & 1, wn = (wid >> 1) & 1, wk = wid >> 2;
    const int bid = blockIdx.x, xcd = bid & 7, idx = bid >> 3;
    const int rt = 8 * (xcd >> 1) + (idx & 7), pn = 4 * (xcd & 1) + (idx >> 3);
    const unsigned lds0 = (unsigned)(uintptr_t)lds;
    unsigned long long ts0 = 0ull, tr0 = 0ull, tA0, tA1;
    const int sr = lane >> 3, spos = lane & 7;
    const unsigned voffB = ((sr & 3) + 16 * (sr >> 2)) * 2048 + ((spos ^ (((sr >> 1) & 1) << 2)) * 16);
    const unsigned voffA = lane * 16;
    const char* pa = (const char*)Ob + ((size_t)(rt * 4 + (wid & 3)) * 32 + (wid >> 2)) * 1024;
    const char* pb = (const char*)wo + (size_t)(4 * (wid & 3)) * 2048 + pn * 256 + (wid >> 2) * 128;
#define K5_ISSUE(s) do { const unsigned so_ = lds0 + ((s) % 6) * 24576; \
        glds16s(pa + (size_t)(s) * 2048, voffA, so_ + (wid >> 2) * 12288 + (wid & 3) * 1024); \
        glds16s(pb + (size_t)(s) * 64 * 2048, voffB, so_ + 4096 + wid * 1024); \
        glds16s(pb + (size_t)(s) * 64 * 2048 + 32 * 2048, voffB, so_ + 12288 + 4096 + wid * 1024); } while (0)
    const int r32 = lane & 31, hi = lane >> 5, fr = lane & 15;
    u32x4 pv[4];
    const float gm = 1.0f - G[rt * 64 + wm * 32 + r32];
#pragma unroll
    for (int s = 0; s < 6; ++s) K5_ISSUE(s);
    f32x16 acc[2] = {};
    const unsigned aX0 = lds0 + wk * 12288 + (wm * 2 + (r32 >> 4)) * 1024 + fr * 64 + ((hi ^ ((-(fr >> 2)) & 3)) * 16);
    const int tq = (lane & 15) >> 2, tp = lane & 3;
    const unsigned aW0 = lds0 + wk * 12288 + 4096 + wn * 4096 + (8 * hi + tq) * 128 + (((tq >> 1) & 1) << 6) + ((lane >> 4) & 1) * 32 + tp * 8;
    tA0 = 0ull;
#define K5_COMP(s) do { const unsigned so = ((s) % 6) * 24576; \
        const bf16x8 x0 = ldsb128(LP(aX0 + so)), x1 = ldsb128(LP((aX0 + so) ^ 32)); \
        const s16x4 w0 = vtr(LP(aW0 + so)), w1 = vtr(LP(aW0 + so) + 512), w2 = vtr(LP((aW0 + so) ^ 64)), w3 = vtr(LP((aW0 + so) ^ 64) + 512); \
        const s16x4 w4 = vtr(LP(aW0 + so) + 2048), w5 = vtr(LP(aW0 + so) + 2560), w6 = vtr(LP((aW0 + so) ^ 64) + 2048), w7 = vtr(LP((aW0 + so) ^ 64) + 2560); \
        acc[0] = MFMA32(cat8(w0, w1), x0, acc[0]); acc[1] = MFMA32(cat8(w2, w3), x0, acc[1]); acc[0] = MFMA32(cat8(w4, w5), x1, acc[0]); acc[1] = MFMA32(cat8(w6, w7), x1, acc[1]); } while (0)
#pragma unroll
    for (int s = 0; s < 16; ++s) {
        switch (s == 0 ? 15 : (s <= 10 ? 12 : (15 - s) * 3 + 4)) { case 15: VW(15); break; case 12: VW(12); break; case 16: VW(16); break; case 13: VW(13); break; case 10: VW(10); break; case 7: VW(7); break; default: VW(4); break; }
        if (s >= 1 && s + 5 < 16) K5_ISSUE(s + 5);
        if (s == 10) {
            const int pm = rt >> 1, wmm = rt & 1;
            const size_t blk = ((size_t)pm * 8 + pn) * 8 + (wmm * 2 + wm) * 2 + wn;
            const char* p0 = (const char*)((const u32x4*)P + blk * 256 + wk * 128); const char* p1 = p0 + (size_t)16 * 8 * 8 * 256 * 16;
            asm volatile("global_load_dwordx4 %0, %4, %5\n\tglobal_load_dwordx4 %1, %4, %5 offset:1024\n\tglobal_load_dwordx4 %2, %4, %6\n\tglobal_load_dwordx4 %3, %4, %6 offset:1024"
                         : "=&v"(pv[0]), "=&v"(pv[1]), "=&v"(pv[2]), "=&v"(pv[3]) : "v"(voffA), "s"(p0), "s"(p1) : "memory");
        }
        K5_COMP(s);
    }
    tA1 = 0ull;
    asm volatile("s_waitcnt vmcnt(0) lgkmcnt(0)\n\ts_barrier" : "+v"(pv[0]), "+v"(pv[1]), "+v"(pv[2]), "+v"(pv[3]) :: "memory");
    {
        int lz = threadIdx.x & 63; asm volatile("" : "+v"(lz));
        const int rr = lz & 31, hh = lz >> 5;
        f32x16 give, keep; if (wk) { give = acc[0]; keep = acc[1]; } else { give = acc[1]; keep = acc[0]; }
        LAS f32x4* xs = (LAS f32x4*)(lds + ((wid & 3) * 2 + wk) * 4096) + lz;
#pragma unroll
        for (int g = 0; g < 4; ++g) xs[g * 64] = (f32x4){give[4 * g], give[4 * g + 1], give[4 * g + 2], give[4 * g + 3]};
        asm volatile("s_waitcnt lgkmcnt(0)\n\ts_barrier" ::: "memory");
        const LAS f32x4* xr = (const LAS f32x4*)(lds + ((wid & 3) * 2 + (1 - wk)) * 4096) + lz;
        const int row = rt * 64 + wm * 32 + rr;
#pragma unroll
        for (int g = 0; g < 4; g += 2) {
            const f32x4 v0 = (f32x4){keep[4 * g], keep[4 * g + 1], keep[4 * g + 2], keep[4 * g + 3]} + xr[g * 64] + UNP(pv[g >> 1], pv[2 + (g >> 1)], 0) * gm;
            const f32x4 v1 = (f32x4){keep[4 * g + 4], keep[4 * g + 5], keep[4 * g + 6], keep[4 * g + 7]} + xr[(g + 1) * 64] + UNP(pv[g >> 1], pv[2 + (g >> 1)], 2) * gm;
            const unsigned ax = cvtpk(v0[0], v0[1]), ay = cvtpk(v0[2], v0[3]), bx = cvtpk(v1[0], v1[1]), by = cvtpk(v1[2], v1[3]);
            auto rx = __builtin_amdgcn_permlane32_swap(ax, bx, false, false); auto ry = __builtin_amdgcn_permlane32_swap(ay, by, false, false);
            const u32x4 w = {rx[0], ry[0], rx[1], ry[1]};
            const int col = pn * 128 + wn * 64 + wk * 32 + (g + hh) * 8;
            *(u32x4*)(out + (size_t)row * 1024 + col) = w;
        }
    }
}

__global__ __launch_bounds__(512, 4) void k23_fused(const u16* __restrict__ QKV, const float* __restrict__ KMAX, const float* __restrict__ G, u16* __restrict__ O,
                                                    const u16* __restrict__ H, const u16* __restrict__ wd, const u16* __restrict__ wo, u16* __restrict__ out, float* __restrict__ P,
                                                    unsigned* __restrict__ flags, unsigned long long* stamps)
{
    extern __shared__ __attribute__((aligned(16))) char lds[];
    const int bid = blockIdx.x;
    unsigned* cnt = flags + 256 * 32;
    if (bid < 256) attn_role(QKV, KMAX, G, O, cnt, lds, bid, stamps);
    else gemm_role(H, wd, P, lds, bid, stamps);
}

extern "C" void kernel_launch(void* const* d_in, const int* in_sizes, int n_in, void* d_out, int out_size, void* d_ws, size_t ws_size, hipStream_t stream)
{
    const u16* x = (const u16*)d_in[0]; const u16* wqkv = (const u16*)d_in[1]; const u16* wo = (const u16*)d_in[2];
    const u16* wg = (const u16*)d_in[4]; const u16* wu = (const u16*)d_in[5]; const u16* wd = (const u16*)d_in[6]; const u16* gw = (const u16*)d_in[7];
    char* ws = (char*)d_ws;
    u16* QKV = (u16*)ws;
    u16* H = (u16*)(ws + (16u << 20));
    u16* Ob = (u16*)(ws + (32u << 20));
    float* P = (float*)(ws + (40u << 20));
    float* G = (float*)(ws + (48u << 20));
    float* KM = (float*)(ws + (48u << 20) + 65536);
    unsigned* flags = (unsigned*)(ws + (48u << 20) + 131072);
    hipFuncSetAttribute((const void*)k1_gemm, hipFuncAttributeMaxDynamicSharedMemorySize, K1_LDS);
    hipFuncSetAttribute((const void*)k4_out, hipFuncAttributeMaxDynamicSharedMemorySize, K4_LDS);
    hipFuncSetAttribute((const void*)k23_fused, hipFuncAttributeMaxDynamicSharedMemorySize, K23_LDS);
    hipLaunchKernelGGL(k1_gemm, dim3(256), dim3(512), K1_LDS, stream, x, wqkv, wg, wu, gw, QKV, H, G, KM, flags, wd, wo, (unsigned long long*)nullptr);
    hipLaunchKernelGGL(k23_fused, dim3(512), dim3(512), K23_LDS, stream, QKV, KM, G, Ob, H, wd, wo, (u16*)d_out, P, flags, (unsigned long long*)nullptr);
    hipLaunchKernelGGL(k4_out, dim3(256), dim3(512), K4_LDS, stream, Ob, wo, P, G, (u16*)d_out, (unsigned long long*)nullptr);
}
```

```cpp
#define K1_AUX 2
#define EXH 8
#define NO_TOUCH
#include <hip/hip_runtime.h>
#include <stdint.h>
typedef unsigned short u16;
typedef short bf16x8 __attribute__((ext_vector_type(8)));
typedef short s16x4 __attribute__((ext_vector_type(4)));
typedef float f32x4 __attribute__((ext_vector_type(4)));
typedef float f32x16 __attribute__((ext_vector_type(16)));
typedef unsigned u32x4 __attribute__((ext_vector_type(4)));
#define LAS __attribute__((address_space(3)))
typedef LAS const char* lds_cptr;
typedef short v4i16_t __attribute__((ext_vector_type(4)));
#define SBAR() __builtin_amdgcn_sched_barrier(0)

constexpr int S_ = 2048, D_ = 1024, F_ = 3584, NH_ = 32;
constexpr float QSCALE = 0.17677669529663687f * 1.4426950408889634f;

typedef __bf16 bf16x2_t __attribute__((ext_vector_type(2)));
typedef float f32x2_t __attribute__((ext_vector_type(2)));
__device__ __forceinline__ unsigned cvtpk(float lo, float hi) { const f32x2_t v = {lo, hi}; return __builtin_bit_cast(unsigned, __builtin_convertvector(v, bf16x2_t)); }
__device__ __forceinline__ void glds16s(const void* sbase, unsigned voff, unsigned lds_base) {
    asm volatile("s_mov_b32 m0, %2\n\ts_nop 0\n\tglobal_load_lds_dwordx4 %0, %1"
                 :: "v"(voff), "s"(sbase), "s"(lds_base) : "memory");
}
__device__ __forceinline__ s16x4 vtr(lds_cptr p) { return __builtin_bit_cast(s16x4, __builtin_amdgcn_ds_read_tr16_b64_v4i16((LAS v4i16_t*)p)); }
__device__ __forceinline__ bf16x8 cat8(s16x4 a, s16x4 b) { return (bf16x8){a[0], a[1], a[2], a[3], b[0], b[1], b[2], b[3]}; }
__device__ __forceinline__ bf16x8 ldsb128(lds_cptr p) { return *(const LAS bf16x8*)p; }

__device__ __forceinline__ lds_cptr LP(unsigned a) { return (lds_cptr)(uintptr_t)a; }
template <int X> __device__ __forceinline__ float swz_xor(float v) { return __int_as_float(__builtin_amdgcn_ds_swizzle(__float_as_int(v), (X << 10) | 0x1f)); }
__device__ __forceinline__ float add32(float v) { auto r = __builtin_amdgcn_permlane32_swap(__float_as_uint(v), __float_as_uint(v), false, false); return __uint_as_float(r[0]) + __uint_as_float(r[1]); }
__device__ __forceinline__ float max32(float v) { auto r = __builtin_amdgcn_permlane32_swap(__float_as_uint(v), __float_as_uint(v), false, false); return fmaxf(__uint_as_float(r[0]), __uint_as_float(r[1])); }
constexpr int K1_STAGE = 73728, K1_LDS = 2 * K1_STAGE;

__device__ __forceinline__ float gelu_mul(float g, float u) {
    const float t = g * g;
    const float p = __builtin_fmaf(t, -0.1029432397f, -2.302208198f);
    const float e = __builtin_amdgcn_exp2f(g * p);
    const float r = __builtin_amdgcn_rcpf(1.0f + e);
    return g * u * r;
}

__global__ __launch_bounds__(512, 2) void k1_gemm(const u16* __restrict__ x, const u16* __restrict__ wqkv, const u16* __restrict__ wg, const u16* __restrict__ wu,
                                                  const u16* __restrict__ gw, u16* __restrict__ QKV, u16* __restrict__ H, float* __restrict__ G, float* __restrict__ KMAX, unsigned* __restrict__ flags, const u16* __restrict__ wd, const u16* __restrict__ wo, unsigned long long* stamps)
{
    extern __shared__ __attribute__((aligned(16))) char lds[];
    const int tid = threadIdx.x, lane = tid & 63;
    const int wid = __builtin_amdgcn_readfirstlane(tid >> 6);
    const int wm = wid & 3, wn = wid >> 2;
    const int fr = lane & 15, fq = lane >> 4;
    const int bid = blockIdx.x, xcd = bid & 7, idx = bid >> 3, pm = idx & 7, pn = xcd * 4 + (idx >> 3);
    const bool typeB = pn >= 24;
    const unsigned lds0 = (unsigned)(uintptr_t)lds;
    unsigned long long ts0=0ull, tr0=0ull;

    if (bid == 0) { for (int i = tid; i < 264 * 32; i += 512) flags[i] = 0u; }
    const char* cb0; const char* cb1; const char* cb2; const char* cb3; const char* cb4;
    unsigned ldmid;
    if (!typeB) { cb0 = (const char*)(wg + 128 * pn); cb1 = (const char*)(wu + 128 * pn); cb2 = cb0 + 128; cb3 = cb1 + 128; cb4 = (const char*)(wqkv + 64 * pn); ldmid = F_ * 2; }
    else { const int i = pn - 24; cb0 = (const char*)(wg + 3072 + 64 * i); cb1 = (const char*)(wu + 3072 + 64 * i); cb2 = (const char*)(wqkv + 64 * (24 + 3 * i)); cb3 = cb2 + 128; cb4 = cb2 + 256; ldmid = 3072 * 2; }
    cb0 += (size_t)(8 * wid) * (F_ * 2); cb1 += (size_t)(8 * wid) * (F_ * 2); cb2 += (size_t)(8 * wid) * ldmid; cb3 += (size_t)(8 * wid) * ldmid; cb4 += (size_t)(8 * wid) * (3072 * 2);
    const char* ca = (const char*)x + (size_t)(256 * pm + 8 * wid) * 2048;
    const int sr = lane >> 3, spos = lane & 7;
    const unsigned fB = ((sr >> 1) & 1) | ((wid & 1) << 1);
    const unsigned colB = (((spos >> 1) ^ fB) * 32) + (spos & 1) * 16;
    const unsigned voffG = sr * (F_ * 2) + colB, voffM = sr * ldmid + colB, voffQ = sr * (3072 * 2) + colB;
    const unsigned voffA = sr * 2048 + ((spos ^ (4 * (wid & 1) + (sr >> 1))) * 16);
    const unsigned ldsw = lds0 + wid * 1024;
    float gsig;

#define K1_PIECE(i, sb) do { \
        if ((i) < 4) glds16s(ca + (size_t)(i) * 131072, voffA, (sb) + (i) * 8192); \
        else if ((i) == 4) glds16s(cb0, voffG, (sb) + 32768); \
        else if ((i) == 5) glds16s(cb2, voffM, (sb) + 32768 + 16384); \
        else if ((i) == 6) glds16s(cb1, voffG, (sb) + 32768 + 8192); \
        else if ((i) == 7) glds16s(cb3, voffM, (sb) + 32768 + 24576); \
        else glds16s(cb4, voffQ, (sb) + 32768 + 32768); } while (0)
#define K1_ADV1() do { ca += 128; cb0 += 64 * F_ * 2; cb2 += (size_t)64 * ldmid; } while (0)
#define K1_ADV2() do { cb1 += 64 * F_ * 2; cb3 += (size_t)64 * ldmid; cb4 += 64 * 3072 * 2; } while (0)

    const int grow = 256 * pm + 8 * pn + wid;
    u32x4 gx0, gx1, gg0, gg1;
    {
        const char* xr = (const char*)(x + (size_t)grow * D_);
        const unsigned go = lane * 32;
        asm volatile("global_load_dwordx4 %0, %4, %5\n\tglobal_load_dwordx4 %1, %4, %5 offset:16\n\tglobal_load_dwordx4 %2, %4, %6\n\tglobal_load_dwordx4 %3, %4, %6 offset:16"
                     : "=&v"(gx0), "=&v"(gx1), "=&v"(gg0), "=&v"(gg1) : "v"(go), "s"(xr), "s"(gw) : "memory");
    }
#pragma unroll
    for (int i = 0; i < 9; ++i) K1_PIECE(i, ldsw);
    K1_ADV1(); K1_ADV2();
#pragma unroll
    for (int i = 0; i < 7; ++i) K1_PIECE(i, ldsw + K1_STAGE);
    K1_ADV1();
    unsigned long long tsa=0ull;
    asm volatile("s_waitcnt vmcnt(16)" : "+v"(gx0), "+v"(gx1), "+v"(gg0), "+v"(gg1) :: "memory");
    {
        float s = 0.f;
#pragma unroll
        for (int j = 0; j < 4; ++j) {
            s += __uint_as_float(gx0[j] << 16) * __uint_as_float(gg0[j] << 16) + __uint_as_float(gx0[j] & 0xffff0000u) * __uint_as_float(gg0[j] & 0xffff0000u);
            s += __uint_as_float(gx1[j] << 16) * __uint_as_float(gg1[j] << 16) + __uint_as_float(gx1[j] & 0xffff0000u) * __uint_as_float(gg1[j] & 0xffff0000u);
        }
        s += swz_xor<1>(s); s += swz_xor<2>(s); s += swz_xor<4>(s); s += swz_xor<8>(s); s += swz_xor<16>(s); s = add32(s);
        gsig = 1.0f / (1.0f + __expf(-s));
    }

    unsigned long long tsb=0ull;
    unsigned aA0 = lds0 + (64 * wm + fr) * 128 + (((0 + fq) ^ (fr >> 1)) * 16);
    unsigned aA1 = lds0 + (64 * wm + fr) * 128 + (((4 + fq) ^ (fr >> 1)) * 16);
    const int tq = (lane & 15) >> 2, tp = lane & 3;
    const unsigned fR = ((tq >> 1) & 1) | ((fq & 1) << 1);
    const unsigned bbase = lds0 + 32768 + (8 * fq + tq) * 128 + 8 * tp;
    unsigned aB0 = bbase + 2 * wn * 8192 + ((0 ^ fR) << 5);
    unsigned aB1 = bbase + 2 * wn * 8192 + ((1 ^ fR) << 5);
    unsigned aB2 = bbase + 2 * wn * 8192 + ((2 ^ fR) << 5);
    unsigned aB3 = bbase + 2 * wn * 8192 + ((3 ^ fR) << 5);
    unsigned aB4a = bbase + 4 * 8192 + (((2 * wn) ^ fR) << 5);
    unsigned aB4b = bbase + 4 * 8192 + (((2 * wn + 1) ^ fR) << 5);

    f32x4 acc[4][10];
#pragma unroll
    for (int m = 0; m < 4; ++m)
#pragma unroll
        for (int s = 0; s < 10; ++s) acc[m][s] = (f32x4){0.f, 0.f, 0.f, 0.f};
    bf16x8 fa0[4], fa1[4];
    s16x4 bx[4], by[4];

#define MF(fb, h, fa, m, ti) acc[m][(ti) + (h)] = __builtin_amdgcn_mfma_f32_16x16x32_bf16(cat8(fb[2 * (h)], fb[2 * (h) + 1]), fa[m], acc[m][(ti) + (h)], 0, 0, 0)
#define RB(dst, j, aa, ab, off) dst[j] = vtr(LP(((j) < 2) ? (aa) : (ab)) + (off) + ((j) & 1) * 512)
#define RA(dst, m, a) dst[m] = ldsb128(LP(a) + (m) * 2048)
#define SUBP(cur, fa, ti, nxt, aa, ab, off, AR, GL) do { \
        MF(cur, 0, fa, 0, ti); SBAR(); RB(nxt, 0, aa, ab, off); SBAR(); \
        MF(cur, 1, fa, 0, ti); SBAR(); RB(nxt, 1, aa, ab, off); SBAR(); \
        MF(cur, 0, fa, 1, ti); SBAR(); RB(nxt, 2, aa, ab, off); SBAR(); \
        MF(cur, 1, fa, 1, ti); SBAR(); RB(nxt, 3, aa, ab, off); SBAR(); \
        MF(cur, 0, fa, 2, ti); SBAR(); AR(0); GL; SBAR(); \
        MF(cur, 1, fa, 2, ti); SBAR(); AR(1); SBAR(); \
        MF(cur, 0, fa, 3, ti); SBAR(); AR(2); SBAR(); \
        MF(cur, 1, fa, 3, ti); SBAR(); AR(3); SBAR(); } while (0)
#define NOA(m) do { } while (0)
#define RA0(m) RA(fa0, m, aA0)
#define RA1(m) RA(fa1, m, aA1)
#define NOG do { } while (0)
#define WBAR(n) asm volatile("s_waitcnt vmcnt(" #n ") lgkmcnt(0)\n\ts_barrier" ::: "memory")

    unsigned warm0 = 0;
    const int wgi = bid * 8 + wid;
    const char* wp = wgi < 1275 ? (const char*)wd + min(wgi * 5760, 7340032 - 5760) : (const char*)wo + min((wgi - 1275) * 5760, 2097152 - 5760);
    const unsigned wvo = (lane % 6) * 64;
    asm volatile("global_load_dword %0, %1, %2" : "+v"(warm0) : "v"(wvo), "s"(wp) : "memory"); wp += 384;
    K1_PIECE(6, ldsw + K1_STAGE);
    asm volatile("s_waitcnt vmcnt(11)\n\ts_barrier" ::: "memory");
    if (lane == 0) G[grow] = gsig;
#pragma unroll
    for (int j = 0; j < 4; ++j) RB(bx, j, aB0, aB1, 0);
#pragma unroll
    for (int m = 0; m < 4; ++m) RA0(m);
    int delta = K1_STAGE;
    unsigned long long ts1=0ull; tsa=0ull;

#define WBARM() WBAR(10)
#define VADD(a) asm volatile("v_add_u32 %0, %1, %0" : "+v"(a) : "s"(delta))
#define STEP(MAIN) do { \
        SUBP(bx, fa0, 0, by, aB0, aB1, 4096, RA1, if (MAIN || p2) K1_PIECE(7, ns)); \
        SUBP(by, fa1, 0, bx, aB2, aB3, 0, NOA, if (MAIN || p2) { K1_PIECE(8, ns); K1_ADV2(); }); \
        SUBP(bx, fa0, 2, by, aB2, aB3, 4096, NOA, NOG); \
        if (MAIN) WBARM(); else if (p2) WBAR(9); else WBAR(0); \
        SUBP(by, fa1, 2, bx, aB0, aB1, 8192, NOA, if (MAIN) K1_PIECE(0, cs)); \
        SUBP(bx, fa0, 4, by, aB0, aB1, 8192 + 4096, NOA, if (MAIN) K1_PIECE(1, cs)); \
        SUBP(by, fa1, 4, bx, aB2, aB3, 8192, NOA, if (MAIN) K1_PIECE(2, cs)); \
        SUBP(bx, fa0, 6, by, aB2, aB3, 8192 + 4096, NOA, if (MAIN) K1_PIECE(3, cs)); \
        SUBP(by, fa1, 6, bx, aB4a, aB4b, 0, NOA, if (MAIN) K1_PIECE(4, cs)); \
        SUBP(bx, fa0, 8, by, aB4a, aB4b, 4096, NOA, if (MAIN) { K1_PIECE(5, cs); K1_ADV1(); }); \
        VADD(aA0); VADD(aA1); VADD(aB0); VADD(aB1); VADD(aB2); VADD(aB3); \
        if (MAIN) WBARM(); else WBAR(3); \
        SUBP(by, fa1, 8, bx, aB0, aB1, 0, RA0, if (MAIN) K1_PIECE(6, cs)); \
        VADD(aB4a); VADD(aB4b); delta = -delta; { const unsigned tmp = cs; cs = ns; ns = tmp; } \
    } while (0)
    unsigned cs = ldsw, ns = ldsw + K1_STAGE;
#pragma nounroll
    for (int tt = 0; tt < 14; ++tt) {
        const bool p2 = true; STEP(true); asm volatile("" : "+s"(cs), "+s"(ns), "+s"(delta));
        asm volatile("global_load_dword %0, %1, %2" : "+v"(warm0) : "v"(wvo), "s"(wp) : "memory"); wp += 384;
    }
#pragma nounroll
    for (int tt = 0; tt < 2; ++tt) { int t = tt; asm volatile("" : "+s"(t)); const bool p2 = t == 0; STEP(false); asm volatile("" : "+s"(cs), "+s"(ns), "+s"(delta)); }
    unsigned long long ts2=0ull; tsb=0ull;

    const int row0 = 256 * pm + 64 * wm;
    const __amdgpu_buffer_rsrc_t rsH = __builtin_amdgcn_make_buffer_rsrc(H, 0, 2048 * 3584 * 2, 0x00020000), rsQ = __builtin_amdgcn_make_buffer_rsrc(QKV, 0, 96 * 2048 * 64, 0x00020000);
#define ST16(base, rs, off, w) __builtin_amdgcn_raw_buffer_store_b128(w, rs, off, 0, K1_AUX)
    const unsigned swz = ((fq ^ ((-(fr >> 2)) & 3)) * 16) + fr * 64;
    const bool gu = !typeB || wn == 0;
    if (gu) {
        const int hc0 = typeB ? (3072 + 64 * (pn - 24)) : (128 * pn + 64 * wn);
#pragma unroll
        for (int m = 0; m < 4; ++m) {
            const int mt = (row0 >> 4) + m;
#pragma unroll
            for (int i = 0; i < 2; ++i) {
                u32x4 w;
                { const f32x4 g = acc[m][2 * i], u = acc[m][4 + 2 * i];
                  w[0] = cvtpk(gelu_mul(g[0], u[0]), gelu_mul(g[1], u[1])); w[1] = cvtpk(gelu_mul(g[2], u[2]), gelu_mul(g[3], u[3])); }
                { const f32x4 g = acc[m][2 * i + 1], u = acc[m][4 + 2 * i + 1];
                  w[2] = cvtpk(gelu_mul(g[0], u[0]), gelu_mul(g[1], u[1])); w[3] = cvtpk(gelu_mul(g[2], u[2]), gelu_mul(g[3], u[3])); }
                ST16(H, rsH, ((unsigned)mt * 112 + (hc0 >> 5) + i) * 1024 + swz, w);
            }
        }
    }
    auto store_head = [&](int hb, const f32x4 (&ta)[4], const f32x4 (&tb)[4]) {
        const int which = hb >> 5;
        const float sc = which == 0 ? QSCALE : 1.0f;
        float kn = 0.f;
#pragma unroll
        for (int m = 0; m < 4; ++m) {
            const f32x4 a = ta[m] * sc, b = tb[m] * sc;
            u32x4 w; w[0] = cvtpk(a[0], a[1]); w[1] = cvtpk(a[2], a[3]); w[2] = cvtpk(b[0], b[1]); w[3] = cvtpk(b[2], b[3]);
            ST16(QKV, rsQ, ((unsigned)hb * 2048 + row0 + 16 * m + fr) * 64 + fq * 16, w);
            if (which == 1) {
                float n2 = a[0] * a[0] + a[1] * a[1] + a[2] * a[2] + a[3] * a[3] + b[0] * b[0] + b[1] * b[1] + b[2] * b[2] + b[3] * b[3];
                n2 += swz_xor<16>(n2); n2 = add32(n2);
                kn = fmaxf(kn, n2);
            }
        }
        if (which == 1) {
            kn = fmaxf(kn, swz_xor<8>(kn)); kn = fmaxf(kn, swz_xor<4>(kn)); kn = fmaxf(kn, swz_xor<2>(kn)); kn = fmaxf(kn, swz_xor<1>(kn));
            if (lane == 0) KMAX[(hb & 31) * 32 + pm * 4 + wm] = kn;
        }
    };
    {
        const int qc4 = typeB ? (24 + 3 * (pn - 24) + 2) : pn;
        const f32x4 ta[4] = {acc[0][8], acc[1][8], acc[2][8], acc[3][8]};
        const f32x4 tb[4] = {acc[0][9], acc[1][9], acc[2][9], acc[3][9]};
        store_head(2 * qc4 + wn, ta, tb);
    }
    if (!gu) {
        const int qc2 = 24 + 3 * (pn - 24);
#pragma unroll
        for (int c = 0; c < 2; ++c)
#pragma unroll
            for (int hh = 0; hh < 2; ++hh) {
                const f32x4 ta[4] = {acc[0][4 * c + 2 * hh], acc[1][4 * c + 2 * hh], acc[2][4 * c + 2 * hh], acc[3][4 * c + 2 * hh]};
                const f32x4 tb[4] = {acc[0][4 * c + 2 * hh + 1], acc[1][4 * c + 2 * hh + 1], acc[2][4 * c + 2 * hh + 1], acc[3][4 * c + 2 * hh + 1]};
                store_head(2 * (qc2 + c) + hh, ta, tb);
            }
    }
    asm volatile("" :: "v"(warm0));
}

#define MFMA32(a, b, c) __builtin_amdgcn_mfma_f32_32x32x16_bf16(a, b, c, 0, 0, 0)
#define MFMA16(a, b, c) __builtin_amdgcn_mfma_f32_16x16x32_bf16(a, b, c, 0, 0, 0)
constexpr int K2_SLOT = 16384, K2_NS = 4, K2_LDS = K2_SLOT * K2_NS;

__device__ __forceinline__ bf16x8 pack8(const f32x16& p, int b) {
    u32x4 w; w[0] = cvtpk(p[b], p[b + 1]); w[1] = cvtpk(p[b + 2], p[b + 3]); w[2] = cvtpk(p[b + 4], p[b + 5]); w[3] = cvtpk(p[b + 6], p[b + 7]);
    return __builtin_bit_cast(bf16x8, w);
}

#define K2_ISSUE(st) do { glds16s(Kh + (size_t)(st) * 8192, voffK, ldsw + ((st) & 3) * K2_SLOT); glds16s(Vh + (size_t)(st) * 8192, voffV, ldsw + ((st) & 3) * K2_SLOT + 8192); } while (0)
#define RDK(d0, d1, slot, i) do { d0 = ldsb128(LP(aK0 + (slot)) + (i) * 2048); d1 = ldsb128(LP(aK1 + (slot)) + (i) * 2048); } while (0)
#define QK(S, k0, k1) do { S = MFMA32(k0, qf0, negm); S = MFMA32(k1, qf1, S); } while (0)
#define PIN(x) asm volatile("" : "+v"(x))
#define PKA(a, S, i) a += (f32x2_t){S[i], S[(i) + 1]}
#define SUM0 do { PKA(ls0, SCUR, 0); PKA(ls1, SCUR, 2); PKA(ls0, SCUR, 4); PKA(ls1, SCUR, 6); PIN(ls0); PIN(ls1); } while (0)
#define SUM1 do { PKA(ls0, SCUR, 8); PKA(ls1, SCUR, 10); PKA(ls0, SCUR, 12); PKA(ls1, SCUR, 14); PIN(ls0); PIN(ls1); } while (0)
#define SUM0 lsum = MFMA16(sel, p0, lsum)
#define SUM1 lsum2 = MFMA16(sel, p1, lsum2)
#define EX4(S, b) do { S[b] = __builtin_amdgcn_exp2f(S[b]); S[b + 1] = __builtin_amdgcn_exp2f(S[b + 1]); S[b + 2] = __builtin_amdgcn_exp2f(S[b + 2]); S[b + 3] = __builtin_amdgcn_exp2f(S[b + 3]); } while (0)
#define RDV(d, slot, i) do { d[0] = vtr(LP(aV + (slot)) + (i) * 2048); d[1] = vtr(LP(aV + (slot)) + (i) * 2048 + 512); d[2] = vtr(LP(aV + (slot)) + (i) * 2048 + 1024); d[3] = vtr(LP(aV + (slot)) + (i) * 2048 + 1536); } while (0)
#define ITER(SC, SN, k0, k1, n0, n1, vc, vn, kslot, ki, vslot, vi) do { \
        f32x16& SCUR = SC; (void)SCUR; \
        SN = MFMA32(k0, qf0, negm); SBAR(); \
        EX4(SC, 0); PIN(SC); SBAR(); \
        SN = MFMA32(k1, qf1, SN); SBAR(); \
        RDK(n0, n1, kslot, ki); SBAR(); \
        EX4(SC, 4); const bf16x8 p0 = pack8(SC, 0); SBAR(); \
        o = MFMA32(cat8(vc[0], vc[1]), p0, o); SBAR(); \
        EX4(SC, 8); PIN(SC); SBAR(); \
        SUM0; SBAR(); \
        EX4(SC, 12); const bf16x8 p1 = pack8(SC, 8); SBAR(); \
        o = MFMA32(cat8(vc[2], vc[3]), p1, o); SBAR(); \
        RDV(vn, vslot, vi); SBAR(); \
        SUM1; SBAR(); \
    } while (0)
#define WB2(n) asm volatile("s_waitcnt vmcnt(" #n ") lgkmcnt(0)\n\ts_barrier" ::: "memory")
constexpr int K23_LDS = 81920;
#define GEMM_PRIO 0
#define ATT_PRIO 0

__device__ __forceinline__ void attn_role(const u16* __restrict__ QKV, const float* __restrict__ KMAX, const float* __restrict__ G, u16* __restrict__ O, unsigned* __restrict__ cnt, char* lds, int bid, unsigned long long* stamps)
{
    const int tid = threadIdx.x, lane = tid & 63;
    const int wid = __builtin_amdgcn_readfirstlane(tid >> 6);
    const int r32 = lane & 31, hi = lane >> 5;
    const int xcd = bid & 7, idx = bid >> 3, head = 4 * xcd + (idx >> 3), qb = idx & 7;
    const unsigned lds0 = (unsigned)(uintptr_t)lds;
    unsigned long long ts0 = 0ull, tr0 = 0ull;
    if (ATT_PRIO) __builtin_amdgcn_s_setprio(ATT_PRIO);
    const char* Qh = (const char*)QKV + (size_t)head * 131072;
    const char* Kh = (const char*)QKV + (size_t)(32 + head) * 131072 + wid * 1024;
    const char* Vh = (const char*)QKV + (size_t)(64 + head) * 131072 + wid * 1024;
    const unsigned voffK = (lane >> 2) * 64 + (((lane & 3) ^ ((lane >> 4) & 3)) * 16), voffV = lane * 16;
    const unsigned ldsw = lds0 + wid * 1024;
    K2_ISSUE(0); K2_ISSUE(1);
    const int q0 = qb * 256 + wid * 32;
    const bf16x8 qf0 = *(const bf16x8*)(Qh + (size_t)(q0 + r32) * 64 + hi * 16);
    const bf16x8 qf1 = *(const bf16x8*)(Qh + (size_t)(q0 + r32) * 64 + 32 + hi * 16);
    const float kmx = KMAX[head * 32 + r32];
    float qn2 = 0.f;
#pragma unroll
    for (int j = 0; j < 8; ++j) { const float a = __uint_as_float((unsigned)(u16)qf0[j] << 16), b = __uint_as_float((unsigned)(u16)qf1[j] << 16); qn2 += a * a + b * b; }
    qn2 = add32(qn2);
    float km2 = kmx; km2 = fmaxf(km2, swz_xor<1>(km2)); km2 = fmaxf(km2, swz_xor<2>(km2)); km2 = fmaxf(km2, swz_xor<4>(km2)); km2 = fmaxf(km2, swz_xor<8>(km2)); km2 = fmaxf(km2, swz_xor<16>(km2));
    const float mb = sqrtf(qn2 * km2) * 1.01f;
    f32x16 negm;
#pragma unroll
    for (int r = 0; r < 16; ++r) negm[r] = -mb;
    asm volatile("" : "+v"(negm));
    bf16x8 sel;
    { const short one = ((lane & 15) == ((lane >> 4) & 1)) ? (short)0x3F80 : (short)0;
#pragma unroll
      for (int j = 0; j < 8; ++j) sel[j] = one; }
    const unsigned aK0 = lds0 + r32 * 64 + (((0 + hi) ^ ((r32 >> 2) & 3)) * 16);
    const unsigned aK1 = lds0 + r32 * 64 + (((2 + hi) ^ ((r32 >> 2) & 3)) * 16);
    const unsigned aV = lds0 + 8192 + (4 * hi + ((lane & 15) >> 2)) * 64 + ((lane >> 4) & 1) * 32 + (lane & 3) * 8;
    f32x16 o = {}; f32x4 lsum = {0.f, 0.f, 0.f, 0.f}, lsum2 = {0.f, 0.f, 0.f, 0.f}; f32x2_t ls0 = {0.f, 0.f}, ls1 = {0.f, 0.f};
    f32x16 sA, sB;
    bf16x8 kfa0, kfa1, kfb0, kfb1;
    s16x4 va[4], vb[4];
    WB2(2);
    K2_ISSUE(2);
    RDK(kfa0, kfa1, 0, 0); RDK(kfb0, kfb1, 0, 1); RDV(va, 0, 0);
    QK(sA, kfa0, kfa1);
    unsigned long long ts1 = 0ull;
#pragma nounroll
    for (int st = 0; st < 16; ++st) {
        int stv = st; asm volatile("" : "+s"(stv));
        const unsigned cur = (stv & 3) * K2_SLOT, nxt = ((stv + 1) & 3) * K2_SLOT;
        ITER(sA, sB, kfb0, kfb1, kfa0, kfa1, va, vb, cur, 2, cur, 1);
        ITER(sB, sA, kfa0, kfa1, kfb0, kfb1, vb, va, cur, 3, cur, 2);
        if (stv <= 12) { WB2(2); K2_ISSUE(stv + 3); } else if (stv == 13) WB2(2); else WB2(0);
        ITER(sA, sB, kfb0, kfb1, kfa0, kfa1, va, vb, nxt, 0, cur, 3);
        ITER(sB, sA, kfa0, kfa1, kfb0, kfb1, vb, va, nxt, 1, nxt, 0);
    }
    lsum += lsum2;
    unsigned long long ts2 = 0ull;
    int lz = threadIdx.x & 63; asm volatile("" : "+v"(lz));
    const int rr = lz & 31, hh = lz >> 5;
    const float gq = G[q0 + rr];
    const float l0 = __int_as_float(__builtin_amdgcn_ds_bpermute((rr & 15) << 2, __float_as_int(lsum[0]))), l1 = __int_as_float(__builtin_amdgcn_ds_bpermute((rr & 15) << 2, __float_as_int(lsum[1])));
    const float l = (rr & 16) ? l1 : l0;
    const float sc = gq / l;
    const int row = q0 + rr, fr = row & 15, mt = row >> 4;
    const __amdgpu_buffer_rsrc_t rs = __builtin_amdgcn_make_buffer_rsrc(O, 0, 2048 * 1024 * 2, 0x00020000);
    const unsigned ob = (unsigned)(mt * 32 + head) * 1024 + fr * 64;
    const int gsw = (-(fr >> 2)) & 3;
#pragma unroll
    for (int c = 0; c < 4; c += 2) {
        unsigned ax = cvtpk(o[4 * c] * sc, o[4 * c + 1] * sc), ay = cvtpk(o[4 * c + 2] * sc, o[4 * c + 3] * sc);
        unsigned bx = cvtpk(o[4 * c + 4] * sc, o[4 * c + 5] * sc), by = cvtpk(o[4 * c + 6] * sc, o[4 * c + 7] * sc);
        auto rx = __builtin_amdgcn_permlane32_swap(ax, bx, false, false); auto ry = __builtin_amdgcn_permlane32_swap(ay, by, false, false);
        const u32x4 w = {rx[0], ry[0], rx[1], ry[1]};
        __builtin_amdgcn_raw_buffer_store_b128(w, rs, ob + (((c + hh) ^ gsw) * 16), 0, 16);
    }
    asm volatile("s_waitcnt vmcnt(0) lgkmcnt(0)\n\ts_barrier" ::: "memory");
    if (threadIdx.x == 0) __hip_atomic_fetch_add(cnt + qb * 32, 1u, __ATOMIC_RELAXED, __HIP_MEMORY_SCOPE_AGENT);
}

#define G_RD(x, w, so, s) do { \
        x[0] = ldsb128(LP((aX0 + (so)) ^ ((s) * 32))); w[0] = vtr(LP(aW0 + (so)) + (s) * 2048); w[1] = vtr(LP(aW0 + (so)) + (s) * 2048 + 512); \
        x[1] = ldsb128(LP((aX0 + (so)) ^ ((s) * 32)) + 2048); w[2] = vtr(LP((aW0 + (so)) ^ 64) + (s) * 2048); w[3] = vtr(LP((aW0 + (so)) ^ 64) + (s) * 2048 + 512); } while (0)
#define G_MM(x, w) do { \
        acc[0][0] = MFMA32(cat8(w[0], w[1]), x[0], acc[0][0]); acc[1][0] = MFMA32(cat8(w[0], w[1]), x[1], acc[1][0]); \
        acc[0][1] = MFMA32(cat8(w[2], w[3]), x[0], acc[0][1]); acc[1][1] = MFMA32(cat8(w[2], w[3]), x[1], acc[1][1]); } while (0)
#define DBGADV pa += 1024; pb += 32 * 2048;
#define ND ((112 - EXH) / 2)
#define PFD 6
__device__ __forceinline__ void gemm_role(const u16* __restrict__ H, const u16* __restrict__ wd, float* __restrict__ P, unsigned* __restrict__ flags, char* lds, int bid, unsigned long long* stamps)
{
    const int tid = threadIdx.x, lane = tid & 63;
    const int wid = __builtin_amdgcn_readfirstlane(tid >> 6);
    const int wm = wid & 1, wn = (wid >> 1) & 1, wk = wid >> 2;
    const int jb = bid - 256, xcd = jb & 7, idx = jb >> 3;
    const int ks = xcd & 1, pnl = idx >> 3, pml = idx & 7, pn = 4 * ((xcd >> 1) & 1) + pnl, pm = 8 * (xcd >> 2) + pml;
    const unsigned lds0 = (unsigned)(uintptr_t)lds;
    unsigned long long ts0 = 0ull, tr0 = 0ull, tA0, tA1;
    if (GEMM_PRIO) __builtin_amdgcn_s_setprio(GEMM_PRIO);
#define VW(n) asm volatile("s_waitcnt vmcnt(" #n ") lgkmcnt(0)\n\ts_barrier" ::: "memory")
    const int sr = lane >> 3, spos = lane & 7;
    const unsigned voffB = ((sr & 3) + 16 * (sr >> 2)) * 2048 + ((spos ^ (((sr >> 1) & 1) << 2)) * 16);
    const unsigned voffA = lane * 16;
    const unsigned ldsA = lds0 + wid * 1024, ldsB = lds0 + 8192 + wid * 1024;
    const char* pa = (const char*)H + ((size_t)(pm * 8 + wid) * 112 + ND * ks) * 1024;
    const char* pb = (const char*)wd + (size_t)(ND * ks * 32 + 4 * (wid & 3)) * 2048 + pn * 256 + (wid >> 2) * 128;
#define K4_ISSUE() do { const unsigned so_ = slot_i * 16384; glds16s(pa, voffA, ldsA + so_); glds16s(pb, voffB, ldsB + so_); pa += 1024; pb += 32 * 2048; slot_i = (slot_i == 4) ? 0 : slot_i + 1; } while (0)
    const int r32 = lane & 31, hi = lane >> 5, fr = lane & 15;
    const unsigned aX0 = lds0 + (wm * 4 + (r32 >> 4)) * 1024 + fr * 64 + ((hi ^ ((-(fr >> 2)) & 3)) * 16);
    const int tq = (lane & 15) >> 2, tp = lane & 3;
    const unsigned aW0 = lds0 + 8192 + wn * 4096 + (8 * hi + tq) * 128 + (((tq >> 1) & 1) << 6) + ((lane >> 4) & 1) * 32 + tp * 8;
    f32x16 acc[2][2];
#pragma unroll
    for (int m = 0; m < 2; ++m)
#pragma unroll
        for (int n = 0; n < 2; ++n) acc[m][n] = (f32x16){};
    bf16x8 xp[2] = {}, xq[2] = {}; s16x4 wp[4] = {}, wq[4] = {};
    int slot_i = 0;
#pragma unroll
    for (int i = 0; i < 5; ++i) K4_ISSUE();
    int rs_i = wk;
    VW(6);
    tA0 = 0ull;
    G_RD(xp, wp, rs_i * 16384, 0);
#define G_STEP(W, I1, I2) do { \
        const unsigned so = rs_i * 16384; \
        rs_i += 2; rs_i = (rs_i >= 5) ? rs_i - 5 : rs_i; \
        const unsigned sn = rs_i * 16384; \
        G_RD(xq, wq, so, 1); SBAR(); G_MM(xp, wp); SBAR(); \
        VW(W); \
        G_RD(xp, wp, sn, 0); SBAR(); if (I1) K4_ISSUE(); if (I2) K4_ISSUE(); SBAR(); G_MM(xq, wq); SBAR(); } while (0)
#pragma nounroll
    for (int t = 0; t < ND / 2 - 3; ++t) { asm volatile("" : "+s"(rs_i), "+s"(slot_i)); G_STEP(2, true, true); }
    G_STEP(2, true, false);
    G_STEP(0, false, false);
    G_STEP(0, false, false);
    asm volatile("s_waitcnt lgkmcnt(0)\n\ts_barrier" ::: "memory");
    tA1 = 0ull;
    {
        int lz = threadIdx.x & 63; asm volatile("" : "+v"(lz));
        const int rr = lz & 31, hh = lz >> 5;
        f32x16 give0, give1, keep0, keep1;
        if (wk) { give0 = acc[0][0]; give1 = acc[0][1]; keep0 = acc[1][0]; keep1 = acc[1][1]; }
        else { give0 = acc[1][0]; give1 = acc[1][1]; keep0 = acc[0][0]; keep1 = acc[0][1]; }
        LAS f32x4* xs = (LAS f32x4*)(lds + ((wid & 3) * 2 + wk) * 8192) + lz;
#pragma unroll
        for (int g = 0; g < 4; ++g) {
            xs[g * 64] = (f32x4){give0[4 * g], give0[4 * g + 1], give0[4 * g + 2], give0[4 * g + 3]};
            xs[(4 + g) * 64] = (f32x4){give1[4 * g], give1[4 * g + 1], give1[4 * g + 2], give1[4 * g + 3]};
        }
        asm volatile("s_waitcnt lgkmcnt(0)\n\ts_barrier" ::: "memory");
        const LAS f32x4* xr = (const LAS f32x4*)(lds + ((wid & 3) * 2 + (1 - wk)) * 8192) + lz;
        const __amdgpu_buffer_rsrc_t rsP = __builtin_amdgcn_make_buffer_rsrc(P, 0, 8 << 20, 0x00020000);
        const unsigned pd = ((unsigned)(((ks * 16 + pm) * 8 + pn) * 8 + (wm * 2 + wk) * 2 + wn) * 256 + lz) * 16;
#pragma unroll
        for (int gp = 0; gp < 2; ++gp) {
            const int g = 2 * gp;
            const f32x4 a0 = (f32x4){keep0[4 * g], keep0[4 * g + 1], keep0[4 * g + 2], keep0[4 * g + 3]} + xr[g * 64];
            const f32x4 a1 = (f32x4){keep0[4 * g + 4], keep0[4 * g + 5], keep0[4 * g + 6], keep0[4 * g + 7]} + xr[(g + 1) * 64];
            const f32x4 b0 = (f32x4){keep1[4 * g], keep1[4 * g + 1], keep1[4 * g + 2], keep1[4 * g + 3]} + xr[(4 + g) * 64];
            const f32x4 b1 = (f32x4){keep1[4 * g + 4], keep1[4 * g + 5], keep1[4 * g + 6], keep1[4 * g + 7]} + xr[(4 + g + 1) * 64];
            const u32x4 wa = {cvtpk(a0[0], a0[1]), cvtpk(a0[2], a0[3]), cvtpk(a1[0], a1[1]), cvtpk(a1[2], a1[3])};
            const u32x4 wb = {cvtpk(b0[0], b0[1]), cvtpk(b0[2], b0[3]), cvtpk(b1[0], b1[1]), cvtpk(b1[2], b1[3])};
            __builtin_amdgcn_raw_buffer_store_b128(wa, rsP, pd + gp * 1024, 0, 16);
            __builtin_amdgcn_raw_buffer_store_b128(wb, rsP, pd + (2 + gp) * 1024, 0, 16);
        }
        asm volatile("s_waitcnt vmcnt(0)\n\ts_barrier" ::: "memory");
        if (threadIdx.x == 0) __hip_atomic_fetch_add(flags + (pm * 8 + pn) * 32, 1u, __ATOMIC_RELAXED, __HIP_MEMORY_SCOPE_AGENT);
        (void)rr; (void)hh;
    }
}

#define UNP(a, b, i) ((f32x4){__uint_as_float(a[i] << 16) + __uint_as_float(b[i] << 16), __uint_as_float(a[i] & 0xffff0000u) + __uint_as_float(b[i] & 0xffff0000u), \
                              __uint_as_float(a[(i) + 1] << 16) + __uint_as_float(b[(i) + 1] << 16), __uint_as_float(a[(i) + 1] & 0xffff0000u) + __uint_as_float(b[(i) + 1] & 0xffff0000u)})

__device__ __forceinline__ void wo_role(const u16* __restrict__ H, const u16* __restrict__ wd, const u16* __restrict__ Ob, const u16* __restrict__ wo, const float* __restrict__ P, const float* __restrict__ G, u16* __restrict__ out,
                                        unsigned* __restrict__ flags, unsigned* __restrict__ cnt, char* lds, int bid, unsigned long long* stamps)
{
    int tid = threadIdx.x; asm volatile("" : "+v"(tid));
    const int lane = tid & 63;
    const int wid = __builtin_amdgcn_readfirstlane(tid >> 6);
    const int wm = wid & 1, wn = (wid >> 1) & 1, wk = wid >> 2;
    const int xcd = bid & 7, idx = bid >> 3;
    const int rt = 8 * (xcd >> 1) + (idx & 7), pn = 4 * (xcd & 1) + (idx >> 3);
    const unsigned lds0 = (unsigned)(uintptr_t)lds;
    unsigned long long tw0 = 0ull, tw1, tw2, tw3;
    const int sr = lane >> 3, spos = lane & 7;
    const unsigned voffB = ((sr & 3) + 16 * (sr >> 2)) * 2048 + ((spos ^ (((sr >> 1) & 1) << 2)) * 16);
    const unsigned voffA = lane * 16;
    const char* pa = (const char*)H + ((size_t)(rt * 4 + (wid & 3)) * 112 + (112 - EXH) + (wid >> 2)) * 1024;
    const char* pb = (const char*)wd + (size_t)((112 - EXH) * 32 + 4 * (wid & 3)) * 2048 + pn * 256 + (wid >> 2) * 128;
    const char* paO = (const char*)Ob + ((size_t)(rt * 4 + (wid & 3)) * 32 + (wid >> 2)) * 1024;
    const char* pbO = (const char*)wo + (size_t)(4 * (wid & 3)) * 2048 + pn * 256 + (wid >> 2) * 128;
    int left = EXH / 2, sl = 0, sc = 0;
    const unsigned dA = lds0 + (wid >> 2) * 12288 + (wid & 3) * 1024, dB = lds0 + 4096 + wid * 1024;
#define K5_ISSUE() do { const unsigned so_ = sl * 24576; glds16s(pa, voffA, dA + so_); glds16s(pb, voffB, dB + so_); glds16s(pb + 32 * 2048, voffB, dB + so_ + 12288); \
                        pa += 2048; pb += (size_t)64 * 2048; sl = (sl == 2) ? 0 : sl + 1; --left; pa = (left == 0) ? paO : pa; pb = (left == 0) ? pbO : pb; } while (0)
    unsigned touch = 0;
    K5_ISSUE(); K5_ISSUE(); K5_ISSUE();
    const int r32 = lane & 31, hi = lane >> 5, fr = lane & 15;
    const unsigned aX0 = lds0 + wk * 12288 + (wm * 2 + (r32 >> 4)) * 1024 + fr * 64 + ((hi ^ ((-(fr >> 2)) & 3)) * 16);
    const int tq = (lane & 15) >> 2, tp = lane & 3;
    const unsigned aW0 = lds0 + wk * 12288 + 4096 + wn * 4096 + (8 * hi + tq) * 128 + (((tq >> 1) & 1) << 6) + ((lane >> 4) & 1) * 32 + tp * 8;
    f32x16 acc[2] = {};
#define K5_COMP() do { const unsigned so = sc * 24576; \
        const bf16x8 x0 = ldsb128(LP(aX0 + so)), x1 = ldsb128(LP((aX0 + so) ^ 32)); \
        const s16x4 w0 = vtr(LP(aW0 + so)), w1 = vtr(LP(aW0 + so) + 512), w2 = vtr(LP((aW0 + so) ^ 64)), w3 = vtr(LP((aW0 + so) ^ 64) + 512); \
        const s16x4 w4 = vtr(LP(aW0 + so) + 2048), w5 = vtr(LP(aW0 + so) + 2560), w6 = vtr(LP((aW0 + so) ^ 64) + 2048), w7 = vtr(LP((aW0 + so) ^ 64) + 2560); \
        acc[0] = MFMA32(cat8(w0, w1), x0, acc[0]); acc[1] = MFMA32(cat8(w2, w3), x0, acc[1]); acc[0] = MFMA32(cat8(w4, w5), x1, acc[0]); acc[1] = MFMA32(cat8(w6, w7), x1, acc[1]); \
        sc = (sc == 2) ? 0 : sc + 1; } while (0)
#define K5_RUN(n_) do { _Pragma("nounroll") for (int s2 = 0; s2 < (n_); ++s2) { asm volatile("" : "+s"(sl), "+s"(sc), "+s"(left)); VW(3); K5_ISSUE(); K5_COMP(); } } while (0)
    VW(6); K5_COMP();
    K5_RUN(EXH / 2 - 3);
    if (wid == 0) {
        unsigned spins = 0;
        while (__builtin_amdgcn_readfirstlane(__hip_atomic_load(cnt + (rt >> 2) * 32, __ATOMIC_RELAXED, __HIP_MEMORY_SCOPE_AGENT)) < 32u) { __builtin_amdgcn_s_sleep(2); if (++spins > (1u << 22)) break; }
        __builtin_amdgcn_fence(__ATOMIC_ACQUIRE, "agent");
        asm volatile("s_waitcnt vmcnt(0)" ::: "memory");
    }
    tw1 = 0ull;
    asm volatile("s_waitcnt lgkmcnt(0)\n\ts_barrier" ::: "memory");
    K5_RUN(2);
    {
        int lz0 = threadIdx.x & 31; asm volatile("" : "+v"(lz0));
        const float gm = 1.0f - G[rt * 64 + wm * 32 + lz0];
        acc[0] *= gm; acc[1] *= gm;
    }
    K5_RUN(14);
    VW(3); K5_COMP();
    VW(0); K5_COMP();
    asm volatile("" :: "v"(touch));
    tw2 = 0ull;
    if (wid == 0) {
        unsigned spins = 0;
        while (__builtin_amdgcn_readfirstlane(__hip_atomic_load(flags + ((rt >> 1) * 8 + pn) * 32, __ATOMIC_RELAXED, __HIP_MEMORY_SCOPE_AGENT)) < 2u) { __builtin_amdgcn_s_sleep(1); if (++spins > (1u << 22)) break; }
    }
    int lz = threadIdx.x & 63; asm volatile("" : "+v"(lz));
    const int rr = lz & 31, hh = lz >> 5;
    f32x16 give, keep; if (wk) { give = acc[0]; keep = acc[1]; } else { give = acc[1]; keep = acc[0]; }
    asm volatile("s_waitcnt vmcnt(0) lgkmcnt(0)\n\ts_barrier" ::: "memory");
    tw3 = 0ull;
    const __amdgpu_buffer_rsrc_t rsP = __builtin_amdgcn_make_buffer_rsrc((void*)P, 0, 8 << 20, 0x00020000);
    const unsigned blk = (unsigned)(((rt >> 1) * 8 + pn) * 8 + ((rt & 1) * 2 + wm) * 2 + wn);
    const unsigned p0 = (blk * 256 + wk * 128 + lz) * 16, p1 = p0 + 16 * 8 * 8 * 256 * 16;
    u32x4 pv[4];
    pv[0] = __builtin_amdgcn_raw_buffer_load_b128(rsP, p0, 0, 16); pv[1] = __builtin_amdgcn_raw_buffer_load_b128(rsP, p0 + 1024, 0, 16);
    pv[2] = __builtin_amdgcn_raw_buffer_load_b128(rsP, p1, 0, 16); pv[3] = __builtin_amdgcn_raw_buffer_load_b128(rsP, p1 + 1024, 0, 16);
    const float gm = 1.0f - G[rt * 64 + wm * 32 + rr];
    LAS f32x4* xs = (LAS f32x4*)(lds + ((wid & 3) * 2 + wk) * 4096) + lz;
#pragma unroll
    for (int g = 0; g < 4; ++g) xs[g * 64] = (f32x4){give[4 * g], give[4 * g + 1], give[4 * g + 2], give[4 * g + 3]};
    asm volatile("s_waitcnt lgkmcnt(0)\n\ts_barrier" ::: "memory");
    const LAS f32x4* xr = (const LAS f32x4*)(lds + ((wid & 3) * 2 + (1 - wk)) * 4096) + lz;
    const int row = rt * 64 + wm * 32 + rr;
#pragma unroll
    for (int g = 0; g < 4; g += 2) {
        const f32x4 v0 = (f32x4){keep[4 * g], keep[4 * g + 1], keep[4 * g + 2], keep[4 * g + 3]} + xr[g * 64] + UNP(pv[g >> 1], pv[2 + (g >> 1)], 0) * gm;
        const f32x4 v1 = (f32x4){keep[4 * g + 4], keep[4 * g + 5], keep[4 * g + 6], keep[4 * g + 7]} + xr[(g + 1) * 64] + UNP(pv[g >> 1], pv[2 + (g >> 1)], 2) * gm;
        const unsigned ax = cvtpk(v0[0], v0[1]), ay = cvtpk(v0[2], v0[3]), bx = cvtpk(v1[0], v1[1]), by = cvtpk(v1[2], v1[3]);
        auto rx = __builtin_amdgcn_permlane32_swap(ax, bx, false, false); auto ry = __builtin_amdgcn_permlane32_swap(ay, by, false, false);
        const u32x4 w = {rx[0], ry[0], rx[1], ry[1]};
        const int col = pn * 128 + wn * 64 + wk * 32 + (g + hh) * 8;
        *(u32x4*)(out + (size_t)row * 1024 + col) = w;
    }
}

__global__ __launch_bounds__(512, 4) void k23_fused(const u16* __restrict__ QKV, const float* __restrict__ KMAX, const float* __restrict__ G, u16* __restrict__ O,
                                                    const u16* __restrict__ H, const u16* __restrict__ wd, const u16* __restrict__ wo, u16* __restrict__ out, float* __restrict__ P,
                                                    unsigned* __restrict__ flags, unsigned long long* stamps)
{
    extern __shared__ __attribute__((aligned(16))) char lds[];
    const int bid = blockIdx.x;
    unsigned* cnt = flags + 256 * 32;
    if (bid < 256) { attn_role(QKV, KMAX, G, O, cnt, lds, bid, stamps); wo_role(H, wd, O, wo, P, G, out, flags, cnt, lds, bid, stamps); }
    else gemm_role(H, wd, P, flags, lds, bid, stamps);
}

extern "C" void kernel_launch(void* const* d_in, const int* in_sizes, int n_in, void* d_out, int out_size, void* d_ws, size_t ws_size, hipStream_t stream)
{
    const u16* x = (const u16*)d_in[0]; const u16* wqkv = (const u16*)d_in[1]; const u16* wo = (const u16*)d_in[2];
    const u16* wg = (const u16*)d_in[4]; const u16* wu = (const u16*)d_in[5]; const u16* wd = (const u16*)d_in[6]; const u16* gw = (const u16*)d_in[7];
    char* ws = (char*)d_ws;
    u16* QKV = (u16*)ws;
    u16* H = (u16*)(ws + (16u << 20));
    u16* Ob = (u16*)(ws + (32u << 20));
    float* P = (float*)(ws + (40u << 20));
    float* G = (float*)(ws + (48u << 20));
    float* KM = (float*)(ws + (48u << 20) + 65536);
    unsigned* flags = (unsigned*)(ws + (48u << 20) + 131072);
    hipFuncSetAttribute((const void*)k1_gemm, hipFuncAttributeMaxDynamicSharedMemorySize, K1_LDS);
    hipFuncSetAttribute((const void*)k23_fused, hipFuncAttributeMaxDynamicSharedMemorySize, K23_LDS);
    hipLaunchKernelGGL(k1_gemm, dim3(256), dim3(512), K1_LDS, stream, x, wqkv, wg, wu, gw, QKV, H, G, KM, flags, wd, wo, (unsigned long long*)nullptr);
    hipLaunchKernelGGL(k23_fused, dim3(512), dim3(512), K23_LDS, stream, QKV, KM, G, Ob, H, wd, wo, (u16*)d_out, P, flags, (unsigned long long*)nullptr);
}
```

```cpp
#define K1_AUX 2
#define EXH 8
#define NO_TOUCH
#define ATT_PRIO 1
#include <hip/hip_runtime.h>
#include <stdint.h>
typedef unsigned short u16;
typedef short bf16x8 __attribute__((ext_vector_type(8)));
typedef short s16x4 __attribute__((ext_vector_type(4)));
typedef float f32x4 __attribute__((ext_vector_type(4)));
typedef float f32x16 __attribute__((ext_vector_type(16)));
typedef unsigned u32x4 __attribute__((ext_vector_type(4)));
#define LAS __attribute__((address_space(3)))
typedef LAS const char* lds_cptr;
typedef short v4i16_t __attribute__((ext_vector_type(4)));
#define SBAR() __builtin_amdgcn_sched_barrier(0)

constexpr int S_ = 2048, D_ = 1024, F_ = 3584, NH_ = 32;
constexpr float QSCALE = 0.17677669529663687f * 1.4426950408889634f;

typedef __bf16 bf16x2_t __attribute__((ext_vector_type(2)));
typedef float f32x2_t __attribute__((ext_vector_type(2)));
__device__ __forceinline__ unsigned cvtpk(float lo, float hi) { const f32x2_t v = {lo, hi}; return __builtin_bit_cast(unsigned, __builtin_convertvector(v, bf16x2_t)); }
__device__ __forceinline__ void glds16s(const void* sbase, unsigned voff, unsigned lds_base) {
    asm volatile("s_mov_b32 m0, %2\n\ts_nop 0\n\tglobal_load_lds_dwordx4 %0, %1"
                 :: "v"(voff), "s"(sbase), "s"(lds_base) : "memory");
}
__device__ __forceinline__ s16x4 vtr(lds_cptr p) { return __builtin_bit_cast(s16x4, __builtin_amdgcn_ds_read_tr16_b64_v4i16((LAS v4i16_t*)p)); }
__device__ __forceinline__ bf16x8 cat8(s16x4 a, s16x4 b) { return (bf16x8){a[0], a[1], a[2], a[3], b[0], b[1], b[2], b[3]}; }
__device__ __forceinline__ bf16x8 ldsb128(lds_cptr p) { return *(const LAS bf16x8*)p; }

__device__ __forceinline__ lds_cptr LP(unsigned a) { return (lds_cptr)(uintptr_t)a; }
template <int X> __device__ __forceinline__ float swz_xor(float v) { return __int_as_float(__builtin_amdgcn_ds_swizzle(__float_as_int(v), (X << 10) | 0x1f)); }
__device__ __forceinline__ float add32(float v) { auto r = __builtin_amdgcn_permlane32_swap(__float_as_uint(v), __float_as_uint(v), false, false); return __uint_as_float(r[0]) + __uint_as_float(r[1]); }
__device__ __forceinline__ float max32(float v) { auto r = __builtin_amdgcn_permlane32_swap(__float_as_uint(v), __float_as_uint(v), false, false); return fmaxf(__uint_as_float(r[0]), __uint_as_float(r[1])); }
constexpr int K1_STAGE = 73728, K1_LDS = 2 * K1_STAGE;

__device__ __forceinline__ float gelu_mul(float g, float u) {
    const float t = g * g;
    const float p = __builtin_fmaf(t, -0.1029432397f, -2.302208198f);
    const float e = __builtin_amdgcn_exp2f(g * p);
    const float r = __builtin_amdgcn_rcpf(1.0f + e);
    return g * u * r;
}

__global__ __launch_bounds__(512, 2) void k1_gemm(const u16* __restrict__ x, const u16* __restrict__ wqkv, const u16* __restrict__ wg, const u16* __restrict__ wu,
                                                  const u16* __restrict__ gw, u16* __restrict__ QKV, u16* __restrict__ H, float* __restrict__ G, float* __restrict__ KMAX, unsigned* __restrict__ flags, const u16* __restrict__ wd, const u16* __restrict__ wo, unsigned long long* stamps)
{
    extern __shared__ __attribute__((aligned(16))) char lds[];
    const int tid = threadIdx.x, lane = tid & 63;
    const int wid = __builtin_amdgcn_readfirstlane(tid >> 6);
    const int wm = wid & 3, wn = wid >> 2;
    const int fr = lane & 15, fq = lane >> 4;
    const int bid = blockIdx.x, xcd = bid & 7, idx = bid >> 3, pm = idx & 7, pn = xcd * 4 + (idx >> 3);
    const bool typeB = pn >= 24;
    const unsigned lds0 = (unsigned)(uintptr_t)lds;
    unsigned long long ts0=0ull, tr0=0ull;

    if (bid == 0) { for (int i = tid; i < 264 * 32; i += 512) flags[i] = 0u; }
    const char* cb0; const char* cb1; const char* cb2; const char* cb3; const char* cb4;
    unsigned ldmid;
    if (!typeB) { cb0 = (const char*)(wg + 128 * pn); cb1 = (const char*)(wu + 128 * pn); cb2 = cb0 + 128; cb3 = cb1 + 128; cb4 = (const char*)(wqkv + 64 * pn); ldmid = F_ * 2; }
    else { const int i = pn - 24; cb0 = (const char*)(wg + 3072 + 64 * i); cb1 = (const char*)(wu + 3072 + 64 * i); cb2 = (const char*)(wqkv + 64 * (24 + 3 * i)); cb3 = cb2 + 128; cb4 = cb2 + 256; ldmid = 3072 * 2; }
    cb0 += (size_t)(8 * wid) * (F_ * 2); cb1 += (size_t)(8 * wid) * (F_ * 2); cb2 += (size_t)(8 * wid) * ldmid; cb3 += (size_t)(8 * wid) * ldmid; cb4 += (size_t)(8 * wid) * (3072 * 2);
    const char* ca = (const char*)x + (size_t)(256 * pm + 8 * wid) * 2048;
    const int sr = lane >> 3, spos = lane & 7;
    const unsigned fB = ((sr >> 1) & 1) | ((wid & 1) << 1);
    const unsigned colB = (((spos >> 1) ^ fB) * 32) + (spos & 1) * 16;
    const unsigned voffG = sr * (F_ * 2) + colB, voffM = sr * ldmid + colB, voffQ = sr * (3072 * 2) + colB;
    const unsigned voffA = sr * 2048 + ((spos ^ (4 * (wid & 1) + (sr >> 1))) * 16);
    const unsigned ldsw = lds0 + wid * 1024;
    float gsig;

#define K1_PIECE(i, sb) do { \
        if ((i) < 4) glds16s(ca + (size_t)(i) * 131072, voffA, (sb) + (i) * 8192); \
        else if ((i) == 4) glds16s(cb0, voffG, (sb) + 32768); \
        else if ((i) == 5) glds16s(cb2, voffM, (sb) + 32768 + 16384); \
        else if ((i) == 6) glds16s(cb1, voffG, (sb) + 32768 + 8192); \
        else if ((i) == 7) glds16s(cb3, voffM, (sb) + 32768 + 24576); \
        else glds16s(cb4, voffQ, (sb) + 32768 + 32768); } while (0)
#define K1_ADV1() do { ca += 128; cb0 += 64 * F_ * 2; cb2 += (size_t)64 * ldmid; } while (0)
#define K1_ADV2() do { cb1 += 64 * F_ * 2; cb3 += (size_t)64 * ldmid; cb4 += 64 * 3072 * 2; } while (0)

    const int grow = 256 * pm + 8 * pn + wid;
    u32x4 gx0, gx1, gg0, gg1;
    {
        const char* xr = (const char*)(x + (size_t)grow * D_);
        const unsigned go = lane * 32;
        asm volatile("global_load_dwordx4 %0, %4, %5\n\tglobal_load_dwordx4 %1, %4, %5 offset:16\n\tglobal_load_dwordx4 %2, %4, %6\n\tglobal_load_dwordx4 %3, %4, %6 offset:16"
                     : "=&v"(gx0), "=&v"(gx1), "=&v"(gg0), "=&v"(gg1) : "v"(go), "s"(xr), "s"(gw) : "memory");
    }
#pragma unroll
    for (int i = 0; i < 9; ++i) K1_PIECE(i, ldsw);
    K1_ADV1(); K1_ADV2();
#pragma unroll
    for (int i = 0; i < 6; ++i) K1_PIECE(i, ldsw + K1_STAGE);
    K1_ADV1();
#define GWAIT 15
    unsigned long long tsa=0ull;
#define STR2(x) #x
#define STR(x) STR2(x)
    asm volatile("s_waitcnt vmcnt(" STR(GWAIT) ")" : "+v"(gx0), "+v"(gx1), "+v"(gg0), "+v"(gg1) :: "memory");
    {
        float s = 0.f;
#pragma unroll
        for (int j = 0; j < 4; ++j) {
            s += __uint_as_float(gx0[j] << 16) * __uint_as_float(gg0[j] << 16) + __uint_as_float(gx0[j] & 0xffff0000u) * __uint_as_float(gg0[j] & 0xffff0000u);
            s += __uint_as_float(gx1[j] << 16) * __uint_as_float(gg1[j] << 16) + __uint_as_float(gx1[j] & 0xffff0000u) * __uint_as_float(gg1[j] & 0xffff0000u);
        }
        s += swz_xor<1>(s); s += swz_xor<2>(s); s += swz_xor<4>(s); s += swz_xor<8>(s); s += swz_xor<16>(s); s = add32(s);
        gsig = 1.0f / (1.0f + __expf(-s));
    }

    unsigned long long tsb=0ull;
    unsigned aA0 = lds0 + (64 * wm + fr) * 128 + (((0 + fq) ^ (fr >> 1)) * 16);
    unsigned aA1 = lds0 + (64 * wm + fr) * 128 + (((4 + fq) ^ (fr >> 1)) * 16);
    const int tq = (lane & 15) >> 2, tp = lane & 3;
    const unsigned fR = ((tq >> 1) & 1) | ((fq & 1) << 1);
    const unsigned bbase = lds0 + 32768 + (8 * fq + tq) * 128 + 8 * tp;
    unsigned aB0 = bbase + 2 * wn * 8192 + ((0 ^ fR) << 5);
    unsigned aB1 = bbase + 2 * wn * 8192 + ((1 ^ fR) << 5);
    unsigned aB2 = bbase + 2 * wn * 8192 + ((2 ^ fR) << 5);
    unsigned aB3 = bbase + 2 * wn * 8192 + ((3 ^ fR) << 5);
    unsigned aB4a = bbase + 4 * 8192 + (((2 * wn) ^ fR) << 5);
    unsigned aB4b = bbase + 4 * 8192 + (((2 * wn + 1) ^ fR) << 5);

    f32x4 acc[4][10];
#pragma unroll
    for (int m = 0; m < 4; ++m)
#pragma unroll
        for (int s = 0; s < 10; ++s) acc[m][s] = (f32x4){0.f, 0.f, 0.f, 0.f};
    bf16x8 fa0[4], fa1[4];
    s16x4 bx[4], by[4];

#define MF(fb, h, fa, m, ti) acc[m][(ti) + (h)] = __builtin_amdgcn_mfma_f32_16x16x32_bf16(cat8(fb[2 * (h)], fb[2 * (h) + 1]), fa[m], acc[m][(ti) + (h)], 0, 0, 0)
#define RB(dst, j, aa, ab, off) dst[j] = vtr(LP(((j) < 2) ? (aa) : (ab)) + (off) + ((j) & 1) * 512)
#define RA(dst, m, a) dst[m] = ldsb128(LP(a) + (m) * 2048)
#define SUBP(cur, fa, ti, nxt, aa, ab, off, AR, GL) do { \
        MF(cur, 0, fa, 0, ti); SBAR(); RB(nxt, 0, aa, ab, off); SBAR(); \
        MF(cur, 1, fa, 0, ti); SBAR(); RB(nxt, 1, aa, ab, off); SBAR(); \
        MF(cur, 0, fa, 1, ti); SBAR(); RB(nxt, 2, aa, ab, off); SBAR(); \
        MF(cur, 1, fa, 1, ti); SBAR(); RB(nxt, 3, aa, ab, off); SBAR(); \
        MF(cur, 0, fa, 2, ti); SBAR(); AR(0); GL; SBAR(); \
        MF(cur, 1, fa, 2, ti); SBAR(); AR(1); SBAR(); \
        MF(cur, 0, fa, 3, ti); SBAR(); AR(2); SBAR(); \
        MF(cur, 1, fa, 3, ti); SBAR(); AR(3); SBAR(); } while (0)
#define NOA(m) do { } while (0)
#define RA0(m) RA(fa0, m, aA0)
#define RA1(m) RA(fa1, m, aA1)
#define NOG do { } while (0)
#define WBAR(n) asm volatile("s_waitcnt vmcnt(" #n ") lgkmcnt(0)\n\ts_barrier" ::: "memory")

    unsigned warm0 = 0;
    const int wgi = bid * 8 + wid;
    const char* wp = wgi < 1275 ? (const char*)wd + min(wgi * 5760, 7340032 - 5760) : (const char*)wo + min((wgi - 1275) * 5760, 2097152 - 5760);
    const unsigned wvo = (lane % 6) * 64;
    asm volatile("global_load_dword %0, %1, %2" : "+v"(warm0) : "v"(wvo), "s"(wp) : "memory"); wp += 384;
    K1_PIECE(6, ldsw + K1_STAGE);
    asm volatile("s_waitcnt vmcnt(11)\n\ts_barrier" ::: "memory");
    if (lane == 0) G[grow] = gsig;
#pragma unroll
    for (int j = 0; j < 4; ++j) RB(bx, j, aB0, aB1, 0);
#pragma unroll
    for (int m = 0; m < 4; ++m) RA0(m);
    int delta = K1_STAGE;
    unsigned long long ts1=0ull; tsa=0ull;

#define WBARM() WBAR(10)
#define VADD(a) asm volatile("v_add_u32 %0, %1, %0" : "+v"(a) : "s"(delta))
#define STEP(MAIN) do { \
        SUBP(bx, fa0, 0, by, aB0, aB1, 4096, RA1, if (MAIN || p2) K1_PIECE(7, ns)); \
        SUBP(by, fa1, 0, bx, aB2, aB3, 0, NOA, if (MAIN || p2) { K1_PIECE(8, ns); K1_ADV2(); }); \
        SUBP(bx, fa0, 2, by, aB2, aB3, 4096, NOA, NOG); \
        if (MAIN) WBARM(); else if (p2) WBAR(9); else WBAR(0); \
        SUBP(by, fa1, 2, bx, aB0, aB1, 8192, NOA, if (MAIN) K1_PIECE(0, cs)); \
        SUBP(bx, fa0, 4, by, aB0, aB1, 8192 + 4096, NOA, if (MAIN) K1_PIECE(1, cs)); \
        SUBP(by, fa1, 4, bx, aB2, aB3, 8192, NOA, if (MAIN) K1_PIECE(2, cs)); \
        SUBP(bx, fa0, 6, by, aB2, aB3, 8192 + 4096, NOA, if (MAIN) K1_PIECE(3, cs)); \
        SUBP(by, fa1, 6, bx, aB4a, aB4b, 0, NOA, if (MAIN) K1_PIECE(4, cs)); \
        SUBP(bx, fa0, 8, by, aB4a, aB4b, 4096, NOA, if (MAIN) { K1_PIECE(5, cs); K1_ADV1(); }); \
        VADD(aA0); VADD(aA1); VADD(aB0); VADD(aB1); VADD(aB2); VADD(aB3); \
        if (MAIN) WBARM(); else WBAR(3); \
        SUBP(by, fa1, 8, bx, aB0, aB1, 0, RA0, if (MAIN) K1_PIECE(6, cs)); \
        VADD(aB4a); VADD(aB4b); delta = -delta; { const unsigned tmp = cs; cs = ns; ns = tmp; } \
    } while (0)
    unsigned cs = ldsw, ns = ldsw + K1_STAGE;
#pragma nounroll
    for (int tt = 0; tt < 14; ++tt) {
        const bool p2 = true; STEP(true); asm volatile("" : "+s"(cs), "+s"(ns), "+s"(delta));
        asm volatile("global_load_dword %0, %1, %2" : "+v"(warm0) : "v"(wvo), "s"(wp) : "memory"); wp += 384;
    }
#pragma nounroll
    for (int tt = 0; tt < 2; ++tt) { int t = tt; asm volatile("" : "+s"(t)); const bool p2 = t == 0; STEP(false); asm volatile("" : "+s"(cs), "+s"(ns), "+s"(delta)); }
    unsigned long long ts2=0ull; tsb=0ull;

    const int row0 = 256 * pm + 64 * wm;
    const __amdgpu_buffer_rsrc_t rsH = __builtin_amdgcn_make_buffer_rsrc(H, 0, 2048 * 3584 * 2, 0x00020000), rsQ = __builtin_amdgcn_make_buffer_rsrc(QKV, 0, 96 * 2048 * 64, 0x00020000);
#define ST16(base, rs, off, w) __builtin_amdgcn_raw_buffer_store_b128(w, rs, off, 0, K1_AUX)
    const unsigned swz = ((fq ^ ((-(fr >> 2)) & 3)) * 16) + fr * 64;
    const bool gu = !typeB || wn == 0;
    if (gu) {
        const int hc0 = typeB ? (3072 + 64 * (pn - 24)) : (128 * pn + 64 * wn);
#pragma unroll
        for (int m = 0; m < 4; ++m) {
            const int mt = (row0 >> 4) + m;
#pragma unroll
            for (int i = 0; i < 2; ++i) {
                u32x4 w;
                { const f32x4 g = acc[m][2 * i], u = acc[m][4 + 2 * i];
                  w[0] = cvtpk(gelu_mul(g[0], u[0]), gelu_mul(g[1], u[1])); w[1] = cvtpk(gelu_mul(g[2], u[2]), gelu_mul(g[3], u[3])); }
                { const f32x4 g = acc[m][2 * i + 1], u = acc[m][4 + 2 * i + 1];
                  w[2] = cvtpk(gelu_mul(g[0], u[0]), gelu_mul(g[1], u[1])); w[3] = cvtpk(gelu_mul(g[2], u[2]), gelu_mul(g[3], u[3])); }
                ST16(H, rsH, ((unsigned)mt * 112 + (hc0 >> 5) + i) * 1024 + swz, w);
            }
        }
    }
    auto store_head = [&](int hb, const f32x4 (&ta)[4], const f32x4 (&tb)[4]) {
        const int which = hb >> 5;
        const float sc = which == 0 ? QSCALE : 1.0f;
        float kn = 0.f;
#pragma unroll
        for (int m = 0; m < 4; ++m) {
            const f32x4 a = ta[m] * sc, b = tb[m] * sc;
            u32x4 w; w[0] = cvtpk(a[0], a[1]); w[1] = cvtpk(a[2], a[3]); w[2] = cvtpk(b[0], b[1]); w[3] = cvtpk(b[2], b[3]);
            ST16(QKV, rsQ, ((unsigned)hb * 2048 + row0 + 16 * m + fr) * 64 + fq * 16, w);
            if (which == 1) {
                float n2 = a[0] * a[0] + a[1] * a[1] + a[2] * a[2] + a[3] * a[3] + b[0] * b[0] + b[1] * b[1] + b[2] * b[2] + b[3] * b[3];
                n2 += swz_xor<16>(n2); n2 = add32(n2);
                kn = fmaxf(kn, n2);
            }
        }
        if (which == 1) {
            kn = fmaxf(kn, swz_xor<8>(kn)); kn = fmaxf(kn, swz_xor<4>(kn)); kn = fmaxf(kn, swz_xor<2>(kn)); kn = fmaxf(kn, swz_xor<1>(kn));
            if (lane == 0) KMAX[(hb & 31) * 32 + pm * 4 + wm] = kn;
        }
    };
    {
        const int qc4 = typeB ? (24 + 3 * (pn - 24) + 2) : pn;
        const f32x4 ta[4] = {acc[0][8], acc[1][8], acc[2][8], acc[3][8]};
        const f32x4 tb[4] = {acc[0][9], acc[1][9], acc[2][9], acc[3][9]};
        store_head(2 * qc4 + wn, ta, tb);
    }
    if (!gu) {
        const int qc2 = 24 + 3 * (pn - 24);
#pragma unroll
        for (int c = 0; c < 2; ++c)
#pragma unroll
            for (int hh = 0; hh < 2; ++hh) {
                const f32x4 ta[4] = {acc[0][4 * c + 2 * hh], acc[1][4 * c + 2 * hh], acc[2][4 * c + 2 * hh], acc[3][4 * c + 2 * hh]};
                const f32x4 tb[4] = {acc[0][4 * c + 2 * hh + 1], acc[1][4 * c + 2 * hh + 1], acc[2][4 * c + 2 * hh + 1], acc[3][4 * c + 2 * hh + 1]};
                store_head(2 * (qc2 + c) + hh, ta, tb);
            }
    }
    asm volatile("" :: "v"(warm0));
}

#define MFMA32(a, b, c) __builtin_amdgcn_mfma_f32_32x32x16_bf16(a, b, c, 0, 0, 0)
#define MFMA16(a, b, c) __builtin_amdgcn_mfma_f32_16x16x32_bf16(a, b, c, 0, 0, 0)
constexpr int K2_SLOT = 16384, K2_NS = 4, K2_LDS = K2_SLOT * K2_NS;

__device__ __forceinline__ bf16x8 pack8(const f32x16& p, int b) {
    u32x4 w; w[0] = cvtpk(p[b], p[b + 1]); w[1] = cvtpk(p[b + 2], p[b + 3]); w[2] = cvtpk(p[b + 4], p[b + 5]); w[3] = cvtpk(p[b + 6], p[b + 7]);
    return __builtin_bit_cast(bf16x8, w);
}

#define K2_ISSUE(st) do { glds16s(Kh + (size_t)(st) * 8192, voffK, ldsw + ((st) & 3) * K2_SLOT); glds16s(Vh + (size_t)(st) * 8192, voffV, ldsw + ((st) & 3) * K2_SLOT + 8192); } while (0)
#define RDK(d0, d1, slot, i) do { d0 = ldsb128(LP(aK0 + (slot)) + (i) * 2048); d1 = ldsb128(LP(aK1 + (slot)) + (i) * 2048); } while (0)
#define QK(S, k0, k1) do { S = MFMA32(k0, qf0, negm); S = MFMA32(k1, qf1, S); } while (0)
#define PIN(x) asm volatile("" : "+v"(x))
#define PKA(a, S, i) a += (f32x2_t){S[i], S[(i) + 1]}
#define SUM0 do { PKA(ls0, SCUR, 0); PKA(ls1, SCUR, 2); PKA(ls0, SCUR, 4); PKA(ls1, SCUR, 6); PIN(ls0); PIN(ls1); } while (0)
#define SUM1 do { PKA(ls0, SCUR, 8); PKA(ls1, SCUR, 10); PKA(ls0, SCUR, 12); PKA(ls1, SCUR, 14); PIN(ls0); PIN(ls1); } while (0)
#define SUM0 lsum = MFMA16(sel, p0, lsum)
#define SUM1 lsum2 = MFMA16(sel, p1, lsum2)
#define EX4(S, b) do { S[b] = __builtin_amdgcn_exp2f(S[b]); S[b + 1] = __builtin_amdgcn_exp2f(S[b + 1]); S[b + 2] = __builtin_amdgcn_exp2f(S[b + 2]); S[b + 3] = __builtin_amdgcn_exp2f(S[b + 3]); } while (0)
#define RDV(d, slot, i) do { d[0] = vtr(LP(aV + (slot)) + (i) * 2048); d[1] = vtr(LP(aV + (slot)) + (i) * 2048 + 512); d[2] = vtr(LP(aV + (slot)) + (i) * 2048 + 1024); d[3] = vtr(LP(aV + (slot)) + (i) * 2048 + 1536); } while (0)
#define ITER(SC, SN, k0, k1, n0, n1, vc, vn, kslot, ki, vslot, vi) do { \
        f32x16& SCUR = SC; (void)SCUR; \
        SN = MFMA32(k0, qf0, negm); SBAR(); \
        EX4(SC, 0); PIN(SC); SBAR(); \
        SN = MFMA32(k1, qf1, SN); SBAR(); \
        RDK(n0, n1, kslot, ki); SBAR(); \
        EX4(SC, 4); const bf16x8 p0 = pack8(SC, 0); SBAR(); \
        o = MFMA32(cat8(vc[0], vc[1]), p0, o); SBAR(); \
        EX4(SC, 8); PIN(SC); SBAR(); \
        SUM0; SBAR(); \
        EX4(SC, 12); const bf16x8 p1 = pack8(SC, 8); SBAR(); \
        o = MFMA32(cat8(vc[2], vc[3]), p1, o); SBAR(); \
        RDV(vn, vslot, vi); SBAR(); \
        SUM1; SBAR(); \
    } while (0)
#define WB2(n) asm volatile("s_waitcnt vmcnt(" #n ") lgkmcnt(0)\n\ts_barrier" ::: "memory")
constexpr int K23_LDS = 81920;
#define GEMM_PRIO 0

__device__ __forceinline__ void attn_role(const u16* __restrict__ QKV, const float* __restrict__ KMAX, const float* __restrict__ G, u16* __restrict__ O, unsigned* __restrict__ cnt, char* lds, int bid, unsigned long long* stamps)
{
    const int tid = threadIdx.x, lane = tid & 63;
    const int wid = __builtin_amdgcn_readfirstlane(tid >> 6);
    const int r32 = lane & 31, hi = lane >> 5;
    const int xcd = bid & 7, idx = bid >> 3, head = 4 * xcd + (idx >> 3), qb = idx & 7;
    const unsigned lds0 = (unsigned)(uintptr_t)lds;
    unsigned long long ts0 = 0ull, tr0 = 0ull;
    if (ATT_PRIO) __builtin_amdgcn_s_setprio(ATT_PRIO);
    const char* Qh = (const char*)QKV + (size_t)head * 131072;
    const char* Kh = (const char*)QKV + (size_t)(32 + head) * 131072 + wid * 1024;
    const char* Vh = (const char*)QKV + (size_t)(64 + head) * 131072 + wid * 1024;
    const unsigned voffK = (lane >> 2) * 64 + (((lane & 3) ^ ((lane >> 4) & 3)) * 16), voffV = lane * 16;
    const unsigned ldsw = lds0 + wid * 1024;
    K2_ISSUE(0); K2_ISSUE(1);
    const int q0 = qb * 256 + wid * 32;
    const bf16x8 qf0 = *(const bf16x8*)(Qh + (size_t)(q0 + r32) * 64 + hi * 16);
    const bf16x8 qf1 = *(const bf16x8*)(Qh + (size_t)(q0 + r32) * 64 + 32 + hi * 16);
    const float kmx = KMAX[head * 32 + r32];
    float qn2 = 0.f;
#pragma unroll
    for (int j = 0; j < 8; ++j) { const float a = __uint_as_float((unsigned)(u16)qf0[j] << 16), b = __uint_as_float((unsigned)(u16)qf1[j] << 16); qn2 += a * a + b * b; }
    qn2 = add32(qn2);
    float km2 = kmx; km2 = fmaxf(km2, swz_xor<1>(km2)); km2 = fmaxf(km2, swz_xor<2>(km2)); km2 = fmaxf(km2, swz_xor<4>(km2)); km2 = fmaxf(km2, swz_xor<8>(km2)); km2 = fmaxf(km2, swz_xor<16>(km2));
    const float mb = sqrtf(qn2 * km2) * 1.01f;
    f32x16 negm;
#pragma unroll
    for (int r = 0; r < 16; ++r) negm[r] = -mb;
    asm volatile("" : "+v"(negm));
    bf16x8 sel;
    { const short one = ((lane & 15) == ((lane >> 4) & 1)) ? (short)0x3F80 : (short)0;
#pragma unroll
      for (int j = 0; j < 8; ++j) sel[j] = one; }
    const unsigned aK0 = lds0 + r32 * 64 + (((0 + hi) ^ ((r32 >> 2) & 3)) * 16);
    const unsigned aK1 = lds0 + r32 * 64 + (((2 + hi) ^ ((r32 >> 2) & 3)) * 16);
    const unsigned aV = lds0 + 8192 + (4 * hi + ((lane & 15) >> 2)) * 64 + ((lane >> 4) & 1) * 32 + (lane & 3) * 8;
    f32x16 o = {}; f32x4 lsum = {0.f, 0.f, 0.f, 0.f}, lsum2 = {0.f, 0.f, 0.f, 0.f}; f32x2_t ls0 = {0.f, 0.f}, ls1 = {0.f, 0.f};
    f32x16 sA, sB;
    bf16x8 kfa0, kfa1, kfb0, kfb1;
    s16x4 va[4], vb[4];
    WB2(2);
    K2_ISSUE(2);
    RDK(kfa0, kfa1, 0, 0); RDK(kfb0, kfb1, 0, 1); RDV(va, 0, 0);
    QK(sA, kfa0, kfa1);
    unsigned long long ts1 = 0ull;
#pragma nounroll
    for (int st = 0; st < 16; ++st) {
        int stv = st; asm volatile("" : "+s"(stv));
        const unsigned cur = (stv & 3) * K2_SLOT, nxt = ((stv + 1) & 3) * K2_SLOT;
        ITER(sA, sB, kfb0, kfb1, kfa0, kfa1, va, vb, cur, 2, cur, 1);
        ITER(sB, sA, kfa0, kfa1, kfb0, kfb1, vb, va, cur, 3, cur, 2);
        if (stv <= 12) { WB2(2); K2_ISSUE(stv + 3); } else if (stv == 13) WB2(2); else WB2(0);
        ITER(sA, sB, kfb0, kfb1, kfa0, kfa1, va, vb, nxt, 0, cur, 3);
        ITER(sB, sA, kfa0, kfa1, kfb0, kfb1, vb, va, nxt, 1, nxt, 0);
    }
    lsum += lsum2;
    unsigned long long ts2 = 0ull;
    int lz = threadIdx.x & 63; asm volatile("" : "+v"(lz));
    const int rr = lz & 31, hh = lz >> 5;
    const float gq = G[q0 + rr];
    float l;
    {
        const float l0 = __int_as_float(__builtin_amdgcn_ds_bpermute((rr & 15) << 2, __float_as_int(lsum[0]))), l1 = __int_as_float(__builtin_amdgcn_ds_bpermute((rr & 15) << 2, __float_as_int(lsum[1])));
        l = (rr & 16) ? l1 : l0;
    }
    const bool redo = __any(!(l >= 3.23117427e-27f));
    if (redo) {
        const char* Kg = (const char*)QKV + (size_t)(32 + head) * 131072 + rr * 64 + hh * 16;
        const u16* Vg = (const u16*)((const char*)QKV + (size_t)(64 + head) * 131072) + rr;
        const f32x16 zero = {};
        float m = -INFINITY;
#pragma nounroll
        for (int t = 0; t < 64; ++t) {
            const bf16x8 k0 = *(const bf16x8*)(Kg + (size_t)t * 2048), k1 = *(const bf16x8*)(Kg + (size_t)t * 2048 + 32);
            f32x16 sc2 = MFMA32(k0, qf0, zero); sc2 = MFMA32(k1, qf1, sc2);
#pragma unroll
            for (int r = 0; r < 16; ++r) m = fmaxf(m, sc2[r]);
        }
        m = max32(m);
        f32x16 nm;
#pragma unroll
        for (int r = 0; r < 16; ++r) nm[r] = -m;
        o = zero; float ls = 0.f;
#pragma nounroll
        for (int t = 0; t < 64; ++t) {
            const bf16x8 k0 = *(const bf16x8*)(Kg + (size_t)t * 2048), k1 = *(const bf16x8*)(Kg + (size_t)t * 2048 + 32);
            f32x16 sc2 = MFMA32(k0, qf0, nm); sc2 = MFMA32(k1, qf1, sc2);
#pragma unroll
            for (int r = 0; r < 16; ++r) sc2[r] = __builtin_amdgcn_exp2f(sc2[r]);
            const bf16x8 p0 = pack8(sc2, 0), p1 = pack8(sc2, 8);
            bf16x8 v0, v1;
#pragma unroll
            for (int j = 0; j < 8; ++j) {
                const int key = t * 32 + 8 * (j >> 2) + 4 * hh + (j & 3);
                v0[j] = (short)Vg[(size_t)key * 32]; v1[j] = (short)Vg[(size_t)(key + 16) * 32];
                ls += __uint_as_float((unsigned)(u16)p0[j] << 16) + __uint_as_float((unsigned)(u16)p1[j] << 16);
            }
            o = MFMA32(v0, p0, o); o = MFMA32(v1, p1, o);
        }
        l = add32(ls);
    }
    const float sc = gq / l;
    const int row = q0 + rr, fr = row & 15, mt = row >> 4;
    const __amdgpu_buffer_rsrc_t rs = __builtin_amdgcn_make_buffer_rsrc(O, 0, 2048 * 1024 * 2, 0x00020000);
    const unsigned ob = (unsigned)(mt * 32 + head) * 1024 + fr * 64;
    const int gsw = (-(fr >> 2)) & 3;
#pragma unroll
    for (int c = 0; c < 4; c += 2) {
        unsigned ax = cvtpk(o[4 * c] * sc, o[4 * c + 1] * sc), ay = cvtpk(o[4 * c + 2] * sc, o[4 * c + 3] * sc);
        unsigned bx = cvtpk(o[4 * c + 4] * sc, o[4 * c + 5] * sc), by = cvtpk(o[4 * c + 6] * sc, o[4 * c + 7] * sc);
        auto rx = __builtin_amdgcn_permlane32_swap(ax, bx, false, false); auto ry = __builtin_amdgcn_permlane32_swap(ay, by, false, false);
        const u32x4 w = {rx[0], ry[0], rx[1], ry[1]};
        __builtin_amdgcn_raw_buffer_store_b128(w, rs, ob + (((c + hh) ^ gsw) * 16), 0, 16);
    }
    asm volatile("s_waitcnt vmcnt(0) lgkmcnt(0)\n\ts_barrier" ::: "memory");
    if (threadIdx.x == 0) __hip_atomic_fetch_add(cnt + qb * 32, 1u, __ATOMIC_RELAXED, __HIP_MEMORY_SCOPE_AGENT);
}

#define G_RD(x, w, so, s) do { \
        x[0] = ldsb128(LP((aX0 + (so)) ^ ((s) * 32))); w[0] = vtr(LP(aW0 + (so)) + (s) * 2048); w[1] = vtr(LP(aW0 + (so)) + (s) * 2048 + 512); \
        x[1] = ldsb128(LP((aX0 + (so)) ^ ((s) * 32)) + 2048); w[2] = vtr(LP((aW0 + (so)) ^ 64) + (s) * 2048); w[3] = vtr(LP((aW0 + (so)) ^ 64) + (s) * 2048 + 512); } while (0)
#define G_MM(x, w) do { \
        acc[0][0] = MFMA32(cat8(w[0], w[1]), x[0], acc[0][0]); acc[1][0] = MFMA32(cat8(w[0], w[1]), x[1], acc[1][0]); \
        acc[0][1] = MFMA32(cat8(w[2], w[3]), x[0], acc[0][1]); acc[1][1] = MFMA32(cat8(w[2], w[3]), x[1], acc[1][1]); } while (0)
#define DBGADV pa += 1024; pb += 32 * 2048;
#define ND ((112 - EXH) / 2)
#define PFD 6
__device__ __forceinline__ void gemm_role(const u16* __restrict__ H, const u16* __restrict__ wd, float* __restrict__ P, unsigned* __restrict__ flags, char* lds, int bid, unsigned long long* stamps)
{
    const int tid = threadIdx.x, lane = tid & 63;
    const int wid = __builtin_amdgcn_readfirstlane(tid >> 6);
    const int wm = wid & 1, wn = (wid >> 1) & 1, wk = wid >> 2;
    const int jb = bid - 256, xcd = jb & 7, idx = jb >> 3;
    const int ks = xcd & 1, pnl = idx >> 3, pml = idx & 7, pn = 4 * ((xcd >> 1) & 1) + pnl, pm = 8 * (xcd >> 2) + pml;
    const unsigned lds0 = (unsigned)(uintptr_t)lds;
    unsigned long long ts0 = 0ull, tr0 = 0ull, tA0, tA1;
    if (GEMM_PRIO) __builtin_amdgcn_s_setprio(GEMM_PRIO);
#define VW(n) asm volatile("s_waitcnt vmcnt(" #n ") lgkmcnt(0)\n\ts_barrier" ::: "memory")
    const int sr = lane >> 3, spos = lane & 7;
    const unsigned voffB = ((sr & 3) + 16 * (sr >> 2)) * 2048 + ((spos ^ (((sr >> 1) & 1) << 2)) * 16);
    const unsigned voffA = lane * 16;
    const unsigned ldsA = lds0 + wid * 1024, ldsB = lds0 + 8192 + wid * 1024;
    const char* pa = (const char*)H + ((size_t)(pm * 8 + wid) * 112 + ND * ks) * 1024;
    const char* pb = (const char*)wd + (size_t)(ND * ks * 32 + 4 * (wid & 3)) * 2048 + pn * 256 + (wid >> 2) * 128;
#define K4_ISSUE() do { const unsigned so_ = slot_i * 16384; glds16s(pa, voffA, ldsA + so_); glds16s(pb, voffB, ldsB + so_); pa += 1024; pb += 32 * 2048; slot_i = (slot_i == 4) ? 0 : slot_i + 1; } while (0)
    const int r32 = lane & 31, hi = lane >> 5, fr = lane & 15;
    const unsigned aX0 = lds0 + (wm * 4 + (r32 >> 4)) * 1024 + fr * 64 + ((hi ^ ((-(fr >> 2)) & 3)) * 16);
    const int tq = (lane & 15) >> 2, tp = lane & 3;
    const unsigned aW0 = lds0 + 8192 + wn * 4096 + (8 * hi + tq) * 128 + (((tq >> 1) & 1) << 6) + ((lane >> 4) & 1) * 32 + tp * 8;
    f32x16 acc[2][2];
#pragma unroll
    for (int m = 0; m < 2; ++m)
#pragma unroll
        for (int n = 0; n < 2; ++n) acc[m][n] = (f32x16){};
    bf16x8 xp[2] = {}, xq[2] = {}; s16x4 wp[4] = {}, wq[4] = {};
    int slot_i = 0;
#pragma unroll
    for (int i = 0; i < 5; ++i) K4_ISSUE();
    int rs_i = wk;
    VW(6);
    tA0 = 0ull;
    G_RD(xp, wp, rs_i * 16384, 0);
#define G_STEP(W, I1, I2) do { \
        const unsigned so = rs_i * 16384; \
        rs_i += 2; rs_i = (rs_i >= 5) ? rs_i - 5 : rs_i; \
        const unsigned sn = rs_i * 16384; \
        G_RD(xq, wq, so, 1); SBAR(); G_MM(xp, wp); SBAR(); \
        VW(W); \
        G_RD(xp, wp, sn, 0); SBAR(); if (I1) K4_ISSUE(); if (I2) K4_ISSUE(); SBAR(); G_MM(xq, wq); SBAR(); } while (0)
#pragma nounroll
    for (int t = 0; t < ND / 2 - 3; ++t) { asm volatile("" : "+s"(rs_i), "+s"(slot_i)); G_STEP(2, true, true); }
    G_STEP(2, true, false);
    G_STEP(0, false, false);
    G_STEP(0, false, false);
    asm volatile("s_waitcnt lgkmcnt(0)\n\ts_barrier" ::: "memory");
    tA1 = 0ull;
    {
        int lz = threadIdx.x & 63; asm volatile("" : "+v"(lz));
        const int rr = lz & 31, hh = lz >> 5;
        f32x16 give0, give1, keep0, keep1;
        if (wk) { give0 = acc[0][0]; give1 = acc[0][1]; keep0 = acc[1][0]; keep1 = acc[1][1]; }
        else { give0 = acc[1][0]; give1 = acc[1][1]; keep0 = acc[0][0]; keep1 = acc[0][1]; }
        LAS f32x4* xs = (LAS f32x4*)(lds + ((wid & 3) * 2 + wk) * 8192) + lz;
#pragma unroll
        for (int g = 0; g < 4; ++g) {
            xs[g * 64] = (f32x4){give0[4 * g], give0[4 * g + 1], give0[4 * g + 2], give0[4 * g + 3]};
            xs[(4 + g) * 64] = (f32x4){give1[4 * g], give1[4 * g + 1], give1[4 * g + 2], give1[4 * g + 3]};
        }
        asm volatile("s_waitcnt lgkmcnt(0)\n\ts_barrier" ::: "memory");
        const LAS f32x4* xr = (const LAS f32x4*)(lds + ((wid & 3) * 2 + (1 - wk)) * 8192) + lz;
        const __amdgpu_buffer_rsrc_t rsP = __builtin_amdgcn_make_buffer_rsrc(P, 0, 8 << 20, 0x00020000);
        const unsigned pd = ((unsigned)(((ks * 16 + pm) * 8 + pn) * 8 + (wm * 2 + wk) * 2 + wn) * 256 + lz) * 16;
#pragma unroll
        for (int gp = 0; gp < 2; ++gp) {
            const int g = 2 * gp;
            const f32x4 a0 = (f32x4){keep0[4 * g], keep0[4 * g + 1], keep0[4 * g + 2], keep0[4 * g + 3]} + xr[g * 64];
            const f32x4 a1 = (f32x4){keep0[4 * g + 4], keep0[4 * g + 5], keep0[4 * g + 6], keep0[4 * g + 7]} + xr[(g + 1) * 64];
            const f32x4 b0 = (f32x4){keep1[4 * g], keep1[4 * g + 1], keep1[4 * g + 2], keep1[4 * g + 3]} + xr[(4 + g) * 64];
            const f32x4 b1 = (f32x4){keep1[4 * g + 4], keep1[4 * g + 5], keep1[4 * g + 6], keep1[4 * g + 7]} + xr[(4 + g + 1) * 64];
            const u32x4 wa = {cvtpk(a0[0], a0[1]), cvtpk(a0[2], a0[3]), cvtpk(a1[0], a1[1]), cvtpk(a1[2], a1[3])};
            const u32x4 wb = {cvtpk(b0[0], b0[1]), cvtpk(b0[2], b0[3]), cvtpk(b1[0], b1[1]), cvtpk(b1[2], b1[3])};
            __builtin_amdgcn_raw_buffer_store_b128(wa, rsP, pd + gp * 1024, 0, 16);
            __builtin_amdgcn_raw_buffer_store_b128(wb, rsP, pd + (2 + gp) * 1024, 0, 16);
        }
        asm volatile("s_waitcnt vmcnt(0)\n\ts_barrier" ::: "memory");
        if (threadIdx.x == 0) __hip_atomic_fetch_add(flags + (pm * 8 + pn) * 32, 1u, __ATOMIC_RELAXED, __HIP_MEMORY_SCOPE_AGENT);
        (void)rr; (void)hh;
    }
}

#define UNP(a, b, i) ((f32x4){__uint_as_float(a[i] << 16) + __uint_as_float(b[i] << 16), __uint_as_float(a[i] & 0xffff0000u) + __uint_as_float(b[i] & 0xffff0000u), \
                              __uint_as_float(a[(i) + 1] << 16) + __uint_as_float(b[(i) + 1] << 16), __uint_as_float(a[(i) + 1] & 0xffff0000u) + __uint_as_float(b[(i) + 1] & 0xffff0000u)})

__device__ __forceinline__ void wo_role(const u16* __restrict__ H, const u16* __restrict__ wd, const u16* __restrict__ Ob, const u16* __restrict__ wo, const float* __restrict__ P, const float* __restrict__ G, u16* __restrict__ out,
                                        unsigned* __restrict__ flags, unsigned* __restrict__ cnt, char* lds, int bid, unsigned long long* stamps)
{
    int tid = threadIdx.x; asm volatile("" : "+v"(tid));
    const int lane = tid & 63;
    const int wid = __builtin_amdgcn_readfirstlane(tid >> 6);
    const int wm = wid & 1, wn = (wid >> 1) & 1, wk = wid >> 2;
    const int xcd = bid & 7, idx = bid >> 3;
    const int rt = 8 * (xcd >> 1) + (idx & 7), pn = 4 * (xcd & 1) + (idx >> 3);
    const unsigned lds0 = (unsigned)(uintptr_t)lds;
    unsigned long long tw0 = 0ull, tw1, tw2, tw3;
    const int sr = lane >> 3, spos = lane & 7;
    const unsigned voffB = ((sr & 3) + 16 * (sr >> 2)) * 2048 + ((spos ^ (((sr >> 1) & 1) << 2)) * 16);
    const unsigned voffA = lane * 16;
    const char* pa = (const char*)H + ((size_t)(rt * 4 + (wid & 3)) * 112 + (112 - EXH) + (wid >> 2)) * 1024;
    const char* pb = (const char*)wd + (size_t)((112 - EXH) * 32 + 4 * (wid & 3)) * 2048 + pn * 256 + (wid >> 2) * 128;
    const char* paO = (const char*)Ob + ((size_t)(rt * 4 + (wid & 3)) * 32 + (wid >> 2)) * 1024;
    const char* pbO = (const char*)wo + (size_t)(4 * (wid & 3)) * 2048 + pn * 256 + (wid >> 2) * 128;
    int left = EXH / 2, sl = 0, sc = 0;
    const unsigned dA = lds0 + (wid >> 2) * 12288 + (wid & 3) * 1024, dB = lds0 + 4096 + wid * 1024;
#define K5_ISSUE() do { const unsigned so_ = sl * 24576; glds16s(pa, voffA, dA + so_); glds16s(pb, voffB, dB + so_); glds16s(pb + 32 * 2048, voffB, dB + so_ + 12288); \
                        pa += 2048; pb += (size_t)64 * 2048; sl = (sl == 2) ? 0 : sl + 1; --left; pa = (left == 0) ? paO : pa; pb = (left == 0) ? pbO : pb; } while (0)
    unsigned touch = 0;
    K5_ISSUE(); K5_ISSUE(); K5_ISSUE();
    const int r32 = lane & 31, hi = lane >> 5, fr = lane & 15;
    const unsigned aX0 = lds0 + wk * 12288 + (wm * 2 + (r32 >> 4)) * 1024 + fr * 64 + ((hi ^ ((-(fr >> 2)) & 3)) * 16);
    const int tq = (lane & 15) >> 2, tp = lane & 3;
    const unsigned aW0 = lds0 + wk * 12288 + 4096 + wn * 4096 + (8 * hi + tq) * 128 + (((tq >> 1) & 1) << 6) + ((lane >> 4) & 1) * 32 + tp * 8;
    f32x16 acc[2] = {};
#define K5_COMP() do { const unsigned so = sc * 24576; \
        const bf16x8 x0 = ldsb128(LP(aX0 + so)), x1 = ldsb128(LP((aX0 + so) ^ 32)); \
        const s16x4 w0 = vtr(LP(aW0 + so)), w1 = vtr(LP(aW0 + so) + 512), w2 = vtr(LP((aW0 + so) ^ 64)), w3 = vtr(LP((aW0 + so) ^ 64) + 512); \
        const s16x4 w4 = vtr(LP(aW0 + so) + 2048), w5 = vtr(LP(aW0 + so) + 2560), w6 = vtr(LP((aW0 + so) ^ 64) + 2048), w7 = vtr(LP((aW0 + so) ^ 64) + 2560); \
        acc[0] = MFMA32(cat8(w0, w1), x0, acc[0]); acc[1] = MFMA32(cat8(w2, w3), x0, acc[1]); acc[0] = MFMA32(cat8(w4, w5), x1, acc[0]); acc[1] = MFMA32(cat8(w6, w7), x1, acc[1]); \
        sc = (sc == 2) ? 0 : sc + 1; } while (0)
#define K5_RUN(n_) do { _Pragma("nounroll") for (int s2 = 0; s2 < (n_); ++s2) { asm volatile("" : "+s"(sl), "+s"(sc), "+s"(left)); VW(3); K5_ISSUE(); K5_COMP(); } } while (0)
    VW(6); K5_COMP();
    K5_RUN(EXH / 2 - 3);
    if (wid == 0) {
        unsigned spins = 0;
        while (__builtin_amdgcn_readfirstlane(__hip_atomic_load(cnt + (rt >> 2) * 32, __ATOMIC_RELAXED, __HIP_MEMORY_SCOPE_AGENT)) < 32u) { __builtin_amdgcn_s_sleep(2); if (++spins > (1u << 22)) break; }
        __builtin_amdgcn_fence(__ATOMIC_ACQUIRE, "agent");
        asm volatile("s_waitcnt vmcnt(0)" ::: "memory");
    }
    tw1 = 0ull;
    asm volatile("s_waitcnt lgkmcnt(0)\n\ts_barrier" ::: "memory");
    K5_RUN(2);
    {
        int lz0 = threadIdx.x & 31; asm volatile("" : "+v"(lz0));
        const float gm = 1.0f - G[rt * 64 + wm * 32 + lz0];
        acc[0] *= gm; acc[1] *= gm;
    }
    K5_RUN(14);
    VW(3); K5_COMP();
    VW(0); K5_COMP();
    asm volatile("" :: "v"(touch));
    tw2 = 0ull;
    if (wid == 0) {
        unsigned spins = 0;
        while (__builtin_amdgcn_readfirstlane(__hip_atomic_load(flags + ((rt >> 1) * 8 + pn) * 32, __ATOMIC_RELAXED, __HIP_MEMORY_SCOPE_AGENT)) < 2u) { __builtin_amdgcn_s_sleep(1); if (++spins > (1u << 22)) break; }
    }
    int lz = threadIdx.x & 63; asm volatile("" : "+v"(lz));
    const int rr = lz & 31, hh = lz >> 5;
    f32x16 give, keep; if (wk) { give = acc[0]; keep = acc[1]; } else { give = acc[1]; keep = acc[0]; }
    asm volatile("s_waitcnt vmcnt(0) lgkmcnt(0)\n\ts_barrier" ::: "memory");
    tw3 = 0ull;
    const __amdgpu_buffer_rsrc_t rsP = __builtin_amdgcn_make_buffer_rsrc((void*)P, 0, 8 << 20, 0x00020000);
    const unsigned blk = (unsigned)(((rt >> 1) * 8 + pn) * 8 + ((rt & 1) * 2 + wm) * 2 + wn);
    const unsigned p0 = (blk * 256 + wk * 128 + lz) * 16, p1 = p0 + 16 * 8 * 8 * 256 * 16;
    u32x4 pv[4];
    pv[0] = __builtin_amdgcn_raw_buffer_load_b128(rsP, p0, 0, 16); pv[1] = __builtin_amdgcn_raw_buffer_load_b128(rsP, p0 + 1024, 0, 16);
    pv[2] = __builtin_amdgcn_raw_buffer_load_b128(rsP, p1, 0, 16); pv[3] = __builtin_amdgcn_raw_buffer_load_b128(rsP, p1 + 1024, 0, 16);
    const float gm = 1.0f - G[rt * 64 + wm * 32 + rr];
    LAS f32x4* xs = (LAS f32x4*)(lds + ((wid & 3) * 2 + wk) * 4096) + lz;
#pragma unroll
    for (int g = 0; g < 4; ++g) xs[g * 64] = (f32x4){give[4 * g], give[4 * g + 1], give[4 * g + 2], give[4 * g + 3]};
    asm volatile("s_waitcnt lgkmcnt(0)\n\ts_barrier" ::: "memory");
    const LAS f32x4* xr = (const LAS f32x4*)(lds + ((wid & 3) * 2 + (1 - wk)) * 4096) + lz;
    const int row = rt * 64 + wm * 32 + rr;
#pragma unroll
    for (int g = 0; g < 4; g += 2) {
        const f32x4 v0 = (f32x4){keep[4 * g], keep[4 * g + 1], keep[4 * g + 2], keep[4 * g + 3]} + xr[g * 64] + UNP(pv[g >> 1], pv[2 + (g >> 1)], 0) * gm;
        const f32x4 v1 = (f32x4){keep[4 * g + 4], keep[4 * g + 5], keep[4 * g + 6], keep[4 * g + 7]} + xr[(g + 1) * 64] + UNP(pv[g >> 1], pv[2 + (g >> 1)], 2) * gm;
        const unsigned ax = cvtpk(v0[0], v0[1]), ay = cvtpk(v0[2], v0[3]), bx = cvtpk(v1[0], v1[1]), by = cvtpk(v1[2], v1[3]);
        auto rx = __builtin_amdgcn_permlane32_swap(ax, bx, false, false); auto ry = __builtin_amdgcn_permlane32_swap(ay, by, false, false);
        const u32x4 w = {rx[0], ry[0], rx[1], ry[1]};
        const int col = pn * 128 + wn * 64 + wk * 32 + (g + hh) * 8;
        *(u32x4*)(out + (size_t)row * 1024 + col) = w;
    }
}

__global__ __launch_bounds__(512, 4) void k23_fused(const u16* __restrict__ QKV, const float* __restrict__ KMAX, const float* __restrict__ G, u16* __restrict__ O,
                                                    const u16* __restrict__ H, const u16* __restrict__ wd, const u16* __restrict__ wo, u16* __restrict__ out, float* __restrict__ P,
                                                    unsigned* __restrict__ flags, unsigned long long* stamps)
{
    extern __shared__ __attribute__((aligned(16))) char lds[];
    const int bid = blockIdx.x;
    unsigned* cnt = flags + 256 * 32;
    if (bid < 256) { attn_role(QKV, KMAX, G, O, cnt, lds, bid, stamps); wo_role(H, wd, O, wo, P, G, out, flags, cnt, lds, bid, stamps); }
    else gemm_role(H, wd, P, flags, lds, bid, stamps);
}

extern "C" void kernel_launch(void* const* d_in, const int* in_sizes, int n_in, void* d_out, int out_size, void* d_ws, size_t ws_size, hipStream_t stream)
{
    const u16* x = (const u16*)d_in[0]; const u16* wqkv = (const u16*)d_in[1]; const u16* wo = (const u16*)d_in[2];
    const u16* wg = (const u16*)d_in[4]; const u16* wu = (const u16*)d_in[5]; const u16* wd = (const u16*)d_in[6]; const u16* gw = (const u16*)d_in[7];
    char* ws = (char*)d_ws;
    u16* QKV = (u16*)ws;
    u16* H = (u16*)(ws + (16u << 20));
    u16* Ob = (u16*)(ws + (32u << 20));
    float* P = (float*)(ws + (40u << 20));
    float* G = (float*)(ws + (48u << 20));
    float* KM = (float*)(ws + (48u << 20) + 65536);
    unsigned* flags = (unsigned*)(ws + (48u << 20) + 131072);
    hipFuncSetAttribute((const void*)k1_gemm, hipFuncAttributeMaxDynamicSharedMemorySize, K1_LDS);
    hipFuncSetAttribute((const void*)k23_fused, hipFuncAttributeMaxDynamicSharedMemorySize, K23_LDS);
    hipLaunchKernelGGL(k1_gemm, dim3(256), dim3(512), K1_LDS, stream, x, wqkv, wg, wu, gw, QKV, H, G, KM, flags, wd, wo, (unsigned long long*)nullptr);
    hipLaunchKernelGGL(k23_fused, dim3(512), dim3(512), K23_LDS, stream, QKV, KM, G, Ob, H, wd, wo, (u16*)d_out, P, flags, (unsigned long long*)nullptr);
}
```

```cpp
#define K1_AUX 2
#define EXH 8
#define NO_TOUCH
#define ATT_PRIO 1
#define GELU_PK
#include <hip/hip_runtime.h>
#include <stdint.h>
typedef unsigned short u16;
typedef short bf16x8 __attribute__((ext_vector_type(8)));
typedef short s16x4 __attribute__((ext_vector_type(4)));
typedef float f32x4 __attribute__((ext_vector_type(4)));
typedef float f32x16 __attribute__((ext_vector_type(16)));
typedef unsigned u32x4 __attribute__((ext_vector_type(4)));
#define LAS __attribute__((address_space(3)))
typedef LAS const char* lds_cptr;
typedef short v4i16_t __attribute__((ext_vector_type(4)));
#define SBAR() __builtin_amdgcn_sched_barrier(0)

constexpr int S_ = 2048, D_ = 1024, F_ = 3584, NH_ = 32;
constexpr float QSCALE = 0.17677669529663687f * 1.4426950408889634f;

typedef __bf16 bf16x2_t __attribute__((ext_vector_type(2)));
typedef float f32x2_t __attribute__((ext_vector_type(2)));
__device__ __forceinline__ unsigned cvtpk(float lo, float hi) { const f32x2_t v = {lo, hi}; return __builtin_bit_cast(unsigned, __builtin_convertvector(v, bf16x2_t)); }
__device__ __forceinline__ void glds16s(const void* sbase, unsigned voff, unsigned lds_base) {
    asm volatile("s_mov_b32 m0, %2\n\ts_nop 0\n\tglobal_load_lds_dwordx4 %0, %1"
                 :: "v"(voff), "s"(sbase), "s"(lds_base) : "memory");
}
__device__ __forceinline__ s16x4 vtr(lds_cptr p) { return __builtin_bit_cast(s16x4, __builtin_amdgcn_ds_read_tr16_b64_v4i16((LAS v4i16_t*)p)); }
__device__ __forceinline__ bf16x8 cat8(s16x4 a, s16x4 b) { return (bf16x8){a[0], a[1], a[2], a[3], b[0], b[1], b[2], b[3]}; }
__device__ __forceinline__ bf16x8 ldsb128(lds_cptr p) { return *(const LAS bf16x8*)p; }

__device__ __forceinline__ lds_cptr LP(unsigned a) { return (lds_cptr)(uintptr_t)a; }
template <int X> __device__ __forceinline__ float swz_xor(float v) { return __int_as_float(__builtin_amdgcn_ds_swizzle(__float_as_int(v), (X << 10) | 0x1f)); }
__device__ __forceinline__ float add32(float v) { auto r = __builtin_amdgcn_permlane32_swap(__float_as_uint(v), __float_as_uint(v), false, false); return __uint_as_float(r[0]) + __uint_as_float(r[1]); }
__device__ __forceinline__ float max32(float v) { auto r = __builtin_amdgcn_permlane32_swap(__float_as_uint(v), __float_as_uint(v), false, false); return fmaxf(__uint_as_float(r[0]), __uint_as_float(r[1])); }
constexpr int K1_STAGE = 73728, K1_LDS = 2 * K1_STAGE;

__device__ __forceinline__ float gelu_mul(float g, float u) {
    const float t = g * g;
    const float p = __builtin_fmaf(t, -0.1029432397f, -2.302208198f);
    const float e = __builtin_amdgcn_exp2f(g * p);
    const float r = __builtin_amdgcn_rcpf(1.0f + e);
    return g * u * r;
}

__device__ __forceinline__ unsigned gelu_mul2(float g0, float g1, float u0, float u1) {
    const f32x2_t g = {g0, g1}, u = {u0, u1};
    const f32x2_t t = g * g;
    const f32x2_t p = t * -0.1029432397f + -2.302208198f;
    const f32x2_t z = g * p;
    f32x2_t e; e[0] = __builtin_amdgcn_exp2f(z[0]); e[1] = __builtin_amdgcn_exp2f(z[1]);
    const f32x2_t d = e + 1.0f;
    f32x2_t r; r[0] = __builtin_amdgcn_rcpf(d[0]); r[1] = __builtin_amdgcn_rcpf(d[1]);
    const f32x2_t h = (g * u) * r;
    return __builtin_bit_cast(unsigned, __builtin_convertvector(h, bf16x2_t));
}

__global__ __launch_bounds__(512, 2) void k1_gemm(const u16* __restrict__ x, const u16* __restrict__ wqkv, const u16* __restrict__ wg, const u16* __restrict__ wu,
                                                  const u16* __restrict__ gw, u16* __restrict__ QKV, u16* __restrict__ H, float* __restrict__ G, float* __restrict__ KMAX, unsigned* __restrict__ flags, const u16* __restrict__ wd, const u16* __restrict__ wo, unsigned long long* stamps)
{
    extern __shared__ __attribute__((aligned(16))) char lds[];
    const int tid = threadIdx.x, lane = tid & 63;
    const int wid = __builtin_amdgcn_readfirstlane(tid >> 6);
    const int wm = wid & 3, wn = wid >> 2;
    const int fr = lane & 15, fq = lane >> 4;
    const int bid = blockIdx.x, xcd = bid & 7, idx = bid >> 3, pm = idx & 7, pn = xcd * 4 + (idx >> 3);
    const bool typeB = pn >= 24;
    const unsigned lds0 = (unsigned)(uintptr_t)lds;
    unsigned long long ts0=0ull, tr0=0ull;

    if (bid == 0) { for (int i = tid; i < 264 * 32; i += 512) flags[i] = 0u; }
    const char* cb0; const char* cb1; const char* cb2; const char* cb3; const char* cb4;
    unsigned ldmid;
    if (!typeB) { cb0 = (const char*)(wg + 128 * pn); cb1 = (const char*)(wu + 128 * pn); cb2 = cb0 + 128; cb3 = cb1 + 128; cb4 = (const char*)(wqkv + 64 * pn); ldmid = F_ * 2; }
    else { const int i = pn - 24; cb0 = (const char*)(wg + 3072 + 64 * i); cb1 = (const char*)(wu + 3072 + 64 * i); cb2 = (const char*)(wqkv + 64 * (24 + 3 * i)); cb3 = cb2 + 128; cb4 = cb2 + 256; ldmid = 3072 * 2; }
    cb0 += (size_t)(8 * wid) * (F_ * 2); cb1 += (size_t)(8 * wid) * (F_ * 2); cb2 += (size_t)(8 * wid) * ldmid; cb3 += (size_t)(8 * wid) * ldmid; cb4 += (size_t)(8 * wid) * (3072 * 2);
    const char* ca = (const char*)x + (size_t)(256 * pm + 8 * wid) * 2048;
    const int sr = lane >> 3, spos = lane & 7;
    const unsigned fB = ((sr >> 1) & 1) | ((wid & 1) << 1);
    const unsigned colB = (((spos >> 1) ^ fB) * 32) + (spos & 1) * 16;
    const unsigned voffG = sr * (F_ * 2) + colB, voffM = sr * ldmid + colB, voffQ = sr * (3072 * 2) + colB;
    const unsigned voffA = sr * 2048 + ((spos ^ (4 * (wid & 1) + (sr >> 1))) * 16);
    const unsigned ldsw = lds0 + wid * 1024;
    float gsig;

#define K1_PIECE(i, sb) do { \
        if ((i) < 4) glds16s(ca + (size_t)(i) * 131072, voffA, (sb) + (i) * 8192); \
        else if ((i) == 4) glds16s(cb0, voffG, (sb) + 32768); \
        else if ((i) == 5) glds16s(cb2, voffM, (sb) + 32768 + 16384); \
        else if ((i) == 6) glds16s(cb1, voffG, (sb) + 32768 + 8192); \
        else if ((i) == 7) glds16s(cb3, voffM, (sb) + 32768 + 24576); \
        else glds16s(cb4, voffQ, (sb) + 32768 + 32768); } while (0)
#define K1_ADV1() do { ca += 128; cb0 += 64 * F_ * 2; cb2 += (size_t)64 * ldmid; } while (0)
#define K1_ADV2() do { cb1 += 64 * F_ * 2; cb3 += (size_t)64 * ldmid; cb4 += 64 * 3072 * 2; } while (0)

    const int grow = 256 * pm + 8 * pn + wid;
    u32x4 gx0, gx1, gg0, gg1;
    {
        const char* xr = (const char*)(x + (size_t)grow * D_);
        const unsigned go = lane * 32;
        asm volatile("global_load_dwordx4 %0, %4, %5\n\tglobal_load_dwordx4 %1, %4, %5 offset:16\n\tglobal_load_dwordx4 %2, %4, %6\n\tglobal_load_dwordx4 %3, %4, %6 offset:16"
                     : "=&v"(gx0), "=&v"(gx1), "=&v"(gg0), "=&v"(gg1) : "v"(go), "s"(xr), "s"(gw) : "memory");
    }
#pragma unroll
    for (int i = 0; i < 9; ++i) K1_PIECE(i, ldsw);
    K1_ADV1(); K1_ADV2();
#pragma unroll
    for (int i = 0; i < 6; ++i) K1_PIECE(i, ldsw + K1_STAGE);
    K1_ADV1();
#define GWAIT 15
    unsigned long long tsa=0ull;
#define STR2(x) #x
#define STR(x) STR2(x)
    asm volatile("s_waitcnt vmcnt(" STR(GWAIT) ")" : "+v"(gx0), "+v"(gx1), "+v"(gg0), "+v"(gg1) :: "memory");
    {
        float s = 0.f;
#pragma unroll
        for (int j = 0; j < 4; ++j) {
            s += __uint_as_float(gx0[j] << 16) * __uint_as_float(gg0[j] << 16) + __uint_as_float(gx0[j] & 0xffff0000u) * __uint_as_float(gg0[j] & 0xffff0000u);
            s += __uint_as_float(gx1[j] << 16) * __uint_as_float(gg1[j] << 16) + __uint_as_float(gx1[j] & 0xffff0000u) * __uint_as_float(gg1[j] & 0xffff0000u);
        }
        s += swz_xor<1>(s); s += swz_xor<2>(s); s += swz_xor<4>(s); s += swz_xor<8>(s); s += swz_xor<16>(s); s = add32(s);
        gsig = 1.0f / (1.0f + __expf(-s));
    }

    unsigned long long tsb=0ull;
    unsigned aA0 = lds0 + (64 * wm + fr) * 128 + (((0 + fq) ^ (fr >> 1)) * 16);
    unsigned aA1 = lds0 + (64 * wm + fr) * 128 + (((4 + fq) ^ (fr >> 1)) * 16);
    const int tq = (lane & 15) >> 2, tp = lane & 3;
    const unsigned fR = ((tq >> 1) & 1) | ((fq & 1) << 1);
    const unsigned bbase = lds0 + 32768 + (8 * fq + tq) * 128 + 8 * tp;
    unsigned aB0 = bbase + 2 * wn * 8192 + ((0 ^ fR) << 5);
    unsigned aB1 = bbase + 2 * wn * 8192 + ((1 ^ fR) << 5);
    unsigned aB2 = bbase + 2 * wn * 8192 + ((2 ^ fR) << 5);
    unsigned aB3 = bbase + 2 * wn * 8192 + ((3 ^ fR) << 5);
    unsigned aB4a = bbase + 4 * 8192 + (((2 * wn) ^ fR) << 5);
    unsigned aB4b = bbase + 4 * 8192 + (((2 * wn + 1) ^ fR) << 5);

    f32x4 acc[4][10];
#pragma unroll
    for (int m = 0; m < 4; ++m)
#pragma unroll
        for (int s = 0; s < 10; ++s) acc[m][s] = (f32x4){0.f, 0.f, 0.f, 0.f};
    bf16x8 fa0[4], fa1[4];
    s16x4 bx[4], by[4];

#define MF(fb, h, fa, m, ti) acc[m][(ti) + (h)] = __builtin_amdgcn_mfma_f32_16x16x32_bf16(cat8(fb[2 * (h)], fb[2 * (h) + 1]), fa[m], acc[m][(ti) + (h)], 0, 0, 0)
#define RB(dst, j, aa, ab, off) dst[j] = vtr(LP(((j) < 2) ? (aa) : (ab)) + (off) + ((j) & 1) * 512)
#define RA(dst, m, a) dst[m] = ldsb128(LP(a) + (m) * 2048)
#define SUBP(cur, fa, ti, nxt, aa, ab, off, AR, GL) do { \
        MF(cur, 0, fa, 0, ti); SBAR(); RB(nxt, 0, aa, ab, off); SBAR(); \
        MF(cur, 1, fa, 0, ti); SBAR(); RB(nxt, 1, aa, ab, off); SBAR(); \
        MF(cur, 0, fa, 1, ti); SBAR(); RB(nxt, 2, aa, ab, off); SBAR(); \
        MF(cur, 1, fa, 1, ti); SBAR(); RB(nxt, 3, aa, ab, off); SBAR(); \
        MF(cur, 0, fa, 2, ti); SBAR(); AR(0); GL; SBAR(); \
        MF(cur, 1, fa, 2, ti); SBAR(); AR(1); SBAR(); \
        MF(cur, 0, fa, 3, ti); SBAR(); AR(2); SBAR(); \
        MF(cur, 1, fa, 3, ti); SBAR(); AR(3); SBAR(); } while (0)
#define NOA(m) do { } while (0)
#define RA0(m) RA(fa0, m, aA0)
#define RA1(m) RA(fa1, m, aA1)
#define NOG do { } while (0)
#define WBAR(n) asm volatile("s_waitcnt vmcnt(" #n ") lgkmcnt(0)\n\ts_barrier" ::: "memory")

    unsigned warm0 = 0;
    const int wgi = bid * 8 + wid;
    const char* wp = wgi < 1275 ? (const char*)wd + min(wgi * 5760, 7340032 - 5760) : (const char*)wo + min((wgi - 1275) * 5760, 2097152 - 5760);
    const unsigned wvo = (lane % 6) * 64;
    asm volatile("global_load_dword %0, %1, %2" : "+v"(warm0) : "v"(wvo), "s"(wp) : "memory"); wp += 384;
    K1_PIECE(6, ldsw + K1_STAGE);
    asm volatile("s_waitcnt vmcnt(11)\n\ts_barrier" ::: "memory");
    if (lane == 0) G[grow] = gsig;
#pragma unroll
    for (int j = 0; j < 4; ++j) RB(bx, j, aB0, aB1, 0);
#pragma unroll
    for (int m = 0; m < 4; ++m) RA0(m);
    int delta = K1_STAGE;
    unsigned long long ts1=0ull; tsa=0ull;

#define WBARM() WBAR(10)
#define VADD(a) asm volatile("v_add_u32 %0, %1, %0" : "+v"(a) : "s"(delta))
#define STEP(MAIN) do { \
        SUBP(bx, fa0, 0, by, aB0, aB1, 4096, RA1, if (MAIN || p2) K1_PIECE(7, ns)); \
        SUBP(by, fa1, 0, bx, aB2, aB3, 0, NOA, if (MAIN || p2) { K1_PIECE(8, ns); K1_ADV2(); }); \
        SUBP(bx, fa0, 2, by, aB2, aB3, 4096, NOA, NOG); \
        if (MAIN) WBARM(); else if (p2) WBAR(9); else WBAR(0); \
        SUBP(by, fa1, 2, bx, aB0, aB1, 8192, NOA, if (MAIN) K1_PIECE(0, cs)); \
        SUBP(bx, fa0, 4, by, aB0, aB1, 8192 + 4096, NOA, if (MAIN) K1_PIECE(1, cs)); \
        SUBP(by, fa1, 4, bx, aB2, aB3, 8192, NOA, if (MAIN) K1_PIECE(2, cs)); \
        SUBP(bx, fa0, 6, by, aB2, aB3, 8192 + 4096, NOA, if (MAIN) K1_PIECE(3, cs)); \
        SUBP(by, fa1, 6, bx, aB4a, aB4b, 0, NOA, if (MAIN) K1_PIECE(4, cs)); \
        SUBP(bx, fa0, 8, by, aB4a, aB4b, 4096, NOA, if (MAIN) { K1_PIECE(5, cs); K1_ADV1(); }); \
        VADD(aA0); VADD(aA1); VADD(aB0); VADD(aB1); VADD(aB2); VADD(aB3); \
        if (MAIN) WBARM(); else WBAR(3); \
        SUBP(by, fa1, 8, bx, aB0, aB1, 0, RA0, if (MAIN) K1_PIECE(6, cs)); \
        VADD(aB4a); VADD(aB4b); delta = -delta; { const unsigned tmp = cs; cs = ns; ns = tmp; } \
    } while (0)
    unsigned cs = ldsw, ns = ldsw + K1_STAGE;
#pragma nounroll
    for (int tt = 0; tt < 14; ++tt) {
        const bool p2 = true; STEP(true); asm volatile("" : "+s"(cs), "+s"(ns), "+s"(delta));
        asm volatile("global_load_dword %0, %1, %2" : "+v"(warm0) : "v"(wvo), "s"(wp) : "memory"); wp += 384;
    }
#pragma nounroll
    for (int tt = 0; tt < 2; ++tt) { int t = tt; asm volatile("" : "+s"(t)); const bool p2 = t == 0; STEP(false); asm volatile("" : "+s"(cs), "+s"(ns), "+s"(delta)); }
    unsigned long long ts2=0ull; tsb=0ull;

    const int row0 = 256 * pm + 64 * wm;
    const __amdgpu_buffer_rsrc_t rsH = __builtin_amdgcn_make_buffer_rsrc(H, 0, 2048 * 3584 * 2, 0x00020000), rsQ = __builtin_amdgcn_make_buffer_rsrc(QKV, 0, 96 * 2048 * 64, 0x00020000);
#define ST16(base, rs, off, w) __builtin_amdgcn_raw_buffer_store_b128(w, rs, off, 0, K1_AUX)
    const unsigned swz = ((fq ^ ((-(fr >> 2)) & 3)) * 16) + fr * 64;
    const bool gu = !typeB || wn == 0;
    if (gu) {
        const int hc0 = typeB ? (3072 + 64 * (pn - 24)) : (128 * pn + 64 * wn);
#pragma unroll
        for (int m = 0; m < 4; ++m) {
            const int mt = (row0 >> 4) + m;
#pragma unroll
            for (int i = 0; i < 2; ++i) {
                u32x4 w;
                { const f32x4 g = acc[m][2 * i], u = acc[m][4 + 2 * i];
                  w[0] = gelu_mul2(g[0], g[1], u[0], u[1]); w[1] = gelu_mul2(g[2], g[3], u[2], u[3]); }
                { const f32x4 g = acc[m][2 * i + 1], u = acc[m][4 + 2 * i + 1];
                  w[2] = gelu_mul2(g[0], g[1], u[0], u[1]); w[3] = gelu_mul2(g[2], g[3], u[2], u[3]); }
                ST16(H, rsH, ((unsigned)mt * 112 + (hc0 >> 5) + i) * 1024 + swz, w);
            }
        }
    }
    auto store_head = [&](int hb, const f32x4 (&ta)[4], const f32x4 (&tb)[4]) {
        const int which = hb >> 5;
        const float sc = which == 0 ? QSCALE : 1.0f;
        float kn = 0.f;
#pragma unroll
        for (int m = 0; m < 4; ++m) {
            const f32x4 a = ta[m] * sc, b = tb[m] * sc;
            u32x4 w; w[0] = cvtpk(a[0], a[1]); w[1] = cvtpk(a[2], a[3]); w[2] = cvtpk(b[0], b[1]); w[3] = cvtpk(b[2], b[3]);
            ST16(QKV, rsQ, ((unsigned)hb * 2048 + row0 + 16 * m + fr) * 64 + fq * 16, w);
            if (which == 1) {
                float n2 = a[0] * a[0] + a[1] * a[1] + a[2] * a[2] + a[3] * a[3] + b[0] * b[0] + b[1] * b[1] + b[2] * b[2] + b[3] * b[3];
                n2 += swz_xor<16>(n2); n2 = add32(n2);
                kn = fmaxf(kn, n2);
            }
        }
        if (which == 1) {
            kn = fmaxf(kn, swz_xor<8>(kn)); kn = fmaxf(kn, swz_xor<4>(kn)); kn = fmaxf(kn, swz_xor<2>(kn)); kn = fmaxf(kn, swz_xor<1>(kn));
            if (lane == 0) KMAX[(hb & 31) * 32 + pm * 4 + wm] = kn;
        }
    };
    {
        const int qc4 = typeB ? (24 + 3 * (pn - 24) + 2) : pn;
        const f32x4 ta[4] = {acc[0][8], acc[1][8], acc[2][8], acc[3][8]};
        const f32x4 tb[4] = {acc[0][9], acc[1][9], acc[2][9], acc[3][9]};
        store_head(2 * qc4 + wn, ta, tb);
    }
    if (!gu) {
        const int qc2 = 24 + 3 * (pn - 24);
#pragma unroll
        for (int c = 0; c < 2; ++c)
#pragma unroll
            for (int hh = 0; hh < 2; ++hh) {
                const f32x4 ta[4] = {acc[0][4 * c + 2 * hh], acc[1][4 * c + 2 * hh], acc[2][4 * c + 2 * hh], acc[3][4 * c + 2 * hh]};
                const f32x4 tb[4] = {acc[0][4 * c + 2 * hh + 1], acc[1][4 * c + 2 * hh + 1], acc[2][4 * c + 2 * hh + 1], acc[3][4 * c + 2 * hh + 1]};
                store_head(2 * (qc2 + c) + hh, ta, tb);
            }
    }
    asm volatile("" :: "v"(warm0));
}

#define MFMA32(a, b, c) __builtin_amdgcn_mfma_f32_32x32x16_bf16(a, b, c, 0, 0, 0)
#define MFMA16(a, b, c) __builtin_amdgcn_mfma_f32_16x16x32_bf16(a, b, c, 0, 0, 0)
constexpr int K2_SLOT = 16384, K2_NS = 4, K2_LDS = K2_SLOT * K2_NS;

__device__ __forceinline__ bf16x8 pack8(const f32x16& p, int b) {
    u32x4 w; w[0] = cvtpk(p[b], p[b + 1]); w[1] = cvtpk(p[b + 2], p[b + 3]); w[2] = cvtpk(p[b + 4], p[b + 5]); w[3] = cvtpk(p[b + 6], p[b + 7]);
    return __builtin_bit_cast(bf16x8, w);
}

#define K2_ISSUE(st) do { glds16s(Kh + (size_t)(st) * 8192, voffK, ldsw + ((st) & 3) * K2_SLOT); glds16s(Vh + (size_t)(st) * 8192, voffV, ldsw + ((st) & 3) * K2_SLOT + 8192); } while (0)
#define RDK(d0, d1, slot, i) do { d0 = ldsb128(LP(aK0 + (slot)) + (i) * 2048); d1 = ldsb128(LP(aK1 + (slot)) + (i) * 2048); } while (0)
#define QK(S, k0, k1) do { S = MFMA32(k0, qf0, negm); S = MFMA32(k1, qf1, S); } while (0)
#define PIN(x) asm volatile("" : "+v"(x))
#define PKA(a, S, i) a += (f32x2_t){S[i], S[(i) + 1]}
#define SUM0 do { PKA(ls0, SCUR, 0); PKA(ls1, SCUR, 2); PKA(ls0, SCUR, 4); PKA(ls1, SCUR, 6); PIN(ls0); PIN(ls1); } while (0)
#define SUM1 do { PKA(ls0, SCUR, 8); PKA(ls1, SCUR, 10); PKA(ls0, SCUR, 12); PKA(ls1, SCUR, 14); PIN(ls0); PIN(ls1); } while (0)
#define SUM0 lsum = MFMA16(sel, p0, lsum)
#define SUM1 lsum2 = MFMA16(sel, p1, lsum2)
#define EX4(S, b) do { S[b] = __builtin_amdgcn_exp2f(S[b]); S[b + 1] = __builtin_amdgcn_exp2f(S[b + 1]); S[b + 2] = __builtin_amdgcn_exp2f(S[b + 2]); S[b + 3] = __builtin_amdgcn_exp2f(S[b + 3]); } while (0)
#define RDV(d, slot, i) do { d[0] = vtr(LP(aV + (slot)) + (i) * 2048); d[1] = vtr(LP(aV + (slot)) + (i) * 2048 + 512); d[2] = vtr(LP(aV + (slot)) + (i) * 2048 + 1024); d[3] = vtr(LP(aV + (slot)) + (i) * 2048 + 1536); } while (0)
#define ITER(SC, SN, k0, k1, n0, n1, vc, vn, kslot, ki, vslot, vi) do { \
        f32x16& SCUR = SC; (void)SCUR; \
        SN = MFMA32(k0, qf0, negm); SBAR(); \
        EX4(SC, 0); PIN(SC); SBAR(); \
        SN = MFMA32(k1, qf1, SN); SBAR(); \
        RDK(n0, n1, kslot, ki); SBAR(); \
        EX4(SC, 4); const bf16x8 p0 = pack8(SC, 0); SBAR(); \
        o = MFMA32(cat8(vc[0], vc[1]), p0, o); SBAR(); \
        EX4(SC, 8); PIN(SC); SBAR(); \
        SUM0; SBAR(); \
        EX4(SC, 12); const bf16x8 p1 = pack8(SC, 8); SBAR(); \
        o = MFMA32(cat8(vc[2], vc[3]), p1, o); SBAR(); \
        RDV(vn, vslot, vi); SBAR(); \
        SUM1; SBAR(); \
    } while (0)
#define WB2(n) asm volatile("s_waitcnt vmcnt(" #n ") lgkmcnt(0)\n\ts_barrier" ::: "memory")
constexpr int K23_LDS = 81920;
#define GEMM_PRIO 0

__device__ __forceinline__ void attn_role(const u16* __restrict__ QKV, const float* __restrict__ KMAX, const float* __restrict__ G, u16* __restrict__ O, unsigned* __restrict__ cnt, char* lds, int bid, unsigned long long* stamps)
{
    const int tid = threadIdx.x, lane = tid & 63;
    const int wid = __builtin_amdgcn_readfirstlane(tid >> 6);
    const int r32 = lane & 31, hi = lane >> 5;
    const int xcd = bid & 7, idx = bid >> 3, head = 4 * xcd + (idx >> 3), qb = idx & 7;
    const unsigned lds0 = (unsigned)(uintptr_t)lds;
    unsigned long long ts0 = 0ull, tr0 = 0ull;
    if (ATT_PRIO) __builtin_amdgcn_s_setprio(ATT_PRIO);
    const char* Qh = (const char*)QKV + (size_t)head * 131072;
    const char* Kh = (const char*)QKV + (size_t)(32 + head) * 131072 + wid * 1024;
    const char* Vh = (const char*)QKV + (size_t)(64 + head) * 131072 + wid * 1024;
    const unsigned voffK = (lane >> 2) * 64 + (((lane & 3) ^ ((lane >> 4) & 3)) * 16), voffV = lane * 16;
    const unsigned ldsw = lds0 + wid * 1024;
    K2_ISSUE(0); K2_ISSUE(1);
    const int q0 = qb * 256 + wid * 32;
    const bf16x8 qf0 = *(const bf16x8*)(Qh + (size_t)(q0 + r32) * 64 + hi * 16);
    const bf16x8 qf1 = *(const bf16x8*)(Qh + (size_t)(q0 + r32) * 64 + 32 + hi * 16);
    const float kmx = KMAX[head * 32 + r32];
    float qn2 = 0.f;
#pragma unroll
    for (int j = 0; j < 8; ++j) { const float a = __uint_as_float((unsigned)(u16)qf0[j] << 16), b = __uint_as_float((unsigned)(u16)qf1[j] << 16); qn2 += a * a + b * b; }
    qn2 = add32(qn2);
    float km2 = kmx; km2 = fmaxf(km2, swz_xor<1>(km2)); km2 = fmaxf(km2, swz_xor<2>(km2)); km2 = fmaxf(km2, swz_xor<4>(km2)); km2 = fmaxf(km2, swz_xor<8>(km2)); km2 = fmaxf(km2, swz_xor<16>(km2));
    const float mb = sqrtf(qn2 * km2) * 1.01f;
    f32x16 negm;
#pragma unroll
    for (int r = 0; r < 16; ++r) negm[r] = -mb;
    asm volatile("" : "+v"(negm));
    bf16x8 sel;
    { const short one = ((lane & 15) == ((lane >> 4) & 1)) ? (short)0x3F80 : (short)0;
#pragma unroll
      for (int j = 0; j < 8; ++j) sel[j] = one; }
    const unsigned aK0 = lds0 + r32 * 64 + (((0 + hi) ^ ((r32 >> 2) & 3)) * 16);
    const unsigned aK1 = lds0 + r32 * 64 + (((2 + hi) ^ ((r32 >> 2) & 3)) * 16);
    const unsigned aV = lds0 + 8192 + (4 * hi + ((lane & 15) >> 2)) * 64 + ((lane >> 4) & 1) * 32 + (lane & 3) * 8;
    f32x16 o = {}; f32x4 lsum = {0.f, 0.f, 0.f, 0.f}, lsum2 = {0.f, 0.f, 0.f, 0.f}; f32x2_t ls0 = {0.f, 0.f}, ls1 = {0.f, 0.f};
    f32x16 sA, sB;
    bf16x8 kfa0, kfa1, kfb0, kfb1;
    s16x4 va[4], vb[4];
    WB2(2);
    K2_ISSUE(2);
    RDK(kfa0, kfa1, 0, 0); RDK(kfb0, kfb1, 0, 1); RDV(va, 0, 0);
    QK(sA, kfa0, kfa1);
    unsigned long long ts1 = 0ull;
#pragma nounroll
    for (int st = 0; st < 16; ++st) {
        int stv = st; asm volatile("" : "+s"(stv));
        const unsigned cur = (stv & 3) * K2_SLOT, nxt = ((stv + 1) & 3) * K2_SLOT;
        ITER(sA, sB, kfb0, kfb1, kfa0, kfa1, va, vb, cur, 2, cur, 1);
        ITER(sB, sA, kfa0, kfa1, kfb0, kfb1, vb, va, cur, 3, cur, 2);
        if (stv <= 12) { WB2(2); K2_ISSUE(stv + 3); } else if (stv == 13) WB2(2); else WB2(0);
        ITER(sA, sB, kfb0, kfb1, kfa0, kfa1, va, vb, nxt, 0, cur, 3);
        ITER(sB, sA, kfa0, kfa1, kfb0, kfb1, vb, va, nxt, 1, nxt, 0);
    }
    lsum += lsum2;
    unsigned long long ts2 = 0ull;
    int lz = threadIdx.x & 63; asm volatile("" : "+v"(lz));
    const int rr = lz & 31, hh = lz >> 5;
    const float gq = G[q0 + rr];
    float l;
    {
        const float l0 = __int_as_float(__builtin_amdgcn_ds_bpermute((rr & 15) << 2, __float_as_int(lsum[0]))), l1 = __int_as_float(__builtin_amdgcn_ds_bpermute((rr & 15) << 2, __float_as_int(lsum[1])));
        l = (rr & 16) ? l1 : l0;
    }
    const bool redo = __any(!(l >= 3.23117427e-27f));
    if (redo) {
        const char* Kg = (const char*)QKV + (size_t)(32 + head) * 131072 + rr * 64 + hh * 16;
        const u16* Vg = (const u16*)((const char*)QKV + (size_t)(64 + head) * 131072) + rr;
        const f32x16 zero = {};
        float m = -INFINITY;
#pragma nounroll
        for (int t = 0; t < 64; ++t) {
            const bf16x8 k0 = *(const bf16x8*)(Kg + (size_t)t * 2048), k1 = *(const bf16x8*)(Kg + (size_t)t * 2048 + 32);
            f32x16 sc2 = MFMA32(k0, qf0, zero); sc2 = MFMA32(k1, qf1, sc2);
#pragma unroll
            for (int r = 0; r < 16; ++r) m = fmaxf(m, sc2[r]);
        }
        m = max32(m);
        f32x16 nm;
#pragma unroll
        for (int r = 0; r < 16; ++r) nm[r] = -m;
        o = zero; float ls = 0.f;
#pragma nounroll
        for (int t = 0; t < 64; ++t) {
            const bf16x8 k0 = *(const bf16x8*)(Kg + (size_t)t * 2048), k1 = *(const bf16x8*)(Kg + (size_t)t * 2048 + 32);
            f32x16 sc2 = MFMA32(k0, qf0, nm); sc2 = MFMA32(k1, qf1, sc2);
#pragma unroll
            for (int r = 0; r < 16; ++r) sc2[r] = __builtin_amdgcn_exp2f(sc2[r]);
            const bf16x8 p0 = pack8(sc2, 0), p1 = pack8(sc2, 8);
            bf16x8 v0, v1;
#pragma unroll
            for (int j = 0; j < 8; ++j) {
                const int key = t * 32 + 8 * (j >> 2) + 4 * hh + (j & 3);
                v0[j] = (short)Vg[(size_t)key * 32]; v1[j] = (short)Vg[(size_t)(key + 16) * 32];
                ls += __uint_as_float((unsigned)(u16)p0[j] << 16) + __uint_as_float((unsigned)(u16)p1[j] << 16);
            }
            o = MFMA32(v0, p0, o); o = MFMA32(v1, p1, o);
        }
        l = add32(ls);
    }
    const float sc = gq / l;
    const int row = q0 + rr, fr = row & 15, mt = row >> 4;
    const __amdgpu_buffer_rsrc_t rs = __builtin_amdgcn_make_buffer_rsrc(O, 0, 2048 * 1024 * 2, 0x00020000);
    const unsigned ob = (unsigned)(mt * 32 + head) * 1024 + fr * 64;
    const int gsw = (-(fr >> 2)) & 3;
#pragma unroll
    for (int c = 0; c < 4; c += 2) {
        unsigned ax = cvtpk(o[4 * c] * sc, o[4 * c + 1] * sc), ay = cvtpk(o[4 * c + 2] * sc, o[4 * c + 3] * sc);
        unsigned bx = cvtpk(o[4 * c + 4] * sc, o[4 * c + 5] * sc), by = cvtpk(o[4 * c + 6] * sc, o[4 * c + 7] * sc);
        auto rx = __builtin_amdgcn_permlane32_swap(ax, bx, false, false); auto ry = __builtin_amdgcn_permlane32_swap(ay, by, false, false);
        const u32x4 w = {rx[0], ry[0], rx[1], ry[1]};
        __builtin_amdgcn_raw_buffer_store_b128(w, rs, ob + (((c + hh) ^ gsw) * 16), 0, 16);
    }
    asm volatile("s_waitcnt vmcnt(0) lgkmcnt(0)\n\ts_barrier" ::: "memory");
    if (threadIdx.x == 0) __hip_atomic_fetch_add(cnt + qb * 32, 1u, __ATOMIC_RELAXED, __HIP_MEMORY_SCOPE_AGENT);
}

#define G_RD(x, w, so, s) do { \
        x[0] = ldsb128(LP((aX0 + (so)) ^ ((s) * 32))); w[0] = vtr(LP(aW0 + (so)) + (s) * 2048); w[1] = vtr(LP(aW0 + (so)) + (s) * 2048 + 512); \
        x[1] = ldsb128(LP((aX0 + (so)) ^ ((s) * 32)) + 2048); w[2] = vtr(LP((aW0 + (so)) ^ 64) + (s) * 2048); w[3] = vtr(LP((aW0 + (so)) ^ 64) + (s) * 2048 + 512); } while (0)
#define G_MM(x, w) do { \
        acc[0][0] = MFMA32(cat8(w[0], w[1]), x[0], acc[0][0]); acc[1][0] = MFMA32(cat8(w[0], w[1]), x[1], acc[1][0]); \
        acc[0][1] = MFMA32(cat8(w[2], w[3]), x[0], acc[0][1]); acc[1][1] = MFMA32(cat8(w[2], w[3]), x[1], acc[1][1]); } while (0)
#define DBGADV pa += 1024; pb += 32 * 2048;
#define PRIO_ON
#define PRIO_OFF
#define ND ((112 - EXH) / 2)
#define PFD 6
__device__ __forceinline__ void gemm_role(const u16* __restrict__ H, const u16* __restrict__ wd, float* __restrict__ P, unsigned* __restrict__ flags, char* lds, int bid, unsigned long long* stamps)
{
    const int tid = threadIdx.x, lane = tid & 63;
    const int wid = __builtin_amdgcn_readfirstlane(tid >> 6);
    const int wm = wid & 1, wn = (wid >> 1) & 1, wk = wid >> 2;
    const int jb = bid - 256, xcd = jb & 7, idx = jb >> 3;
    const int ks = xcd & 1, pnl = idx >> 3, pml = idx & 7, pn = 4 * ((xcd >> 1) & 1) + pnl, pm = 8 * (xcd >> 2) + pml;
    const unsigned lds0 = (unsigned)(uintptr_t)lds;
    unsigned long long ts0 = 0ull, tr0 = 0ull, tA0, tA1;
    if (GEMM_PRIO) __builtin_amdgcn_s_setprio(GEMM_PRIO);
#define VW(n) asm volatile("s_waitcnt vmcnt(" #n ") lgkmcnt(0)\n\ts_barrier" ::: "memory")
    const int sr = lane >> 3, spos = lane & 7;
    const unsigned voffB = ((sr & 3) + 16 * (sr >> 2)) * 2048 + ((spos ^ (((sr >> 1) & 1) << 2)) * 16);
    const unsigned voffA = lane * 16;
    const unsigned ldsA = lds0 + wid * 1024, ldsB = lds0 + 8192 + wid * 1024;
    const char* pa = (const char*)H + ((size_t)(pm * 8 + wid) * 112 + ND * ks) * 1024;
    const char* pb = (const char*)wd + (size_t)(ND * ks * 32 + 4 * (wid & 3)) * 2048 + pn * 256 + (wid >> 2) * 128;
#define K4_ISSUE() do { const unsigned so_ = slot_i * 16384; glds16s(pa, voffA, ldsA + so_); glds16s(pb, voffB, ldsB + so_); pa += 1024; pb += 32 * 2048; slot_i = (slot_i == 4) ? 0 : slot_i + 1; } while (0)
    const int r32 = lane & 31, hi = lane >> 5, fr = lane & 15;
    const unsigned aX0 = lds0 + (wm * 4 + (r32 >> 4)) * 1024 + fr * 64 + ((hi ^ ((-(fr >> 2)) & 3)) * 16);
    const int tq = (lane & 15) >> 2, tp = lane & 3;
    const unsigned aW0 = lds0 + 8192 + wn * 4096 + (8 * hi + tq) * 128 + (((tq >> 1) & 1) << 6) + ((lane >> 4) & 1) * 32 + tp * 8;
    f32x16 acc[2][2];
#pragma unroll
    for (int m = 0; m < 2; ++m)
#pragma unroll
        for (int n = 0; n < 2; ++n) acc[m][n] = (f32x16){};
    bf16x8 xp[2] = {}, xq[2] = {}; s16x4 wp[4] = {}, wq[4] = {};
    int slot_i = 0;
#pragma unroll
    for (int i = 0; i < 5; ++i) K4_ISSUE();
    int rs_i = wk;
    VW(6);
    tA0 = 0ull;
    G_RD(xp, wp, rs_i * 16384, 0);
#define G_STEP(W, I1, I2) do { \
        const unsigned so = rs_i * 16384; \
        rs_i += 2; rs_i = (rs_i >= 5) ? rs_i - 5 : rs_i; \
        const unsigned sn = rs_i * 16384; \
        G_RD(xq, wq, so, 1); SBAR(); G_MM(xp, wp); SBAR(); \
        VW(W); \
        G_RD(xp, wp, sn, 0); SBAR(); if (I1) K4_ISSUE(); if (I2) K4_ISSUE(); SBAR(); PRIO_ON; G_MM(xq, wq); PRIO_OFF; SBAR(); } while (0)
#pragma nounroll
    for (int t = 0; t < ND / 2 - 3; ++t) { asm volatile("" : "+s"(rs_i), "+s"(slot_i)); G_STEP(2, true, true); }
    G_STEP(2, true, false);
    G_STEP(0, false, false);
    G_STEP(0, false, false);
    asm volatile("s_waitcnt lgkmcnt(0)\n\ts_barrier" ::: "memory");
    tA1 = 0ull;
    {
        int lz = threadIdx.x & 63; asm volatile("" : "+v"(lz));
        const int rr = lz & 31, hh = lz >> 5;
        f32x16 give0, give1, keep0, keep1;
        if (wk) { give0 = acc[0][0]; give1 = acc[0][1]; keep0 = acc[1][0]; keep1 = acc[1][1]; }
        else { give0 = acc[1][0]; give1 = acc[1][1]; keep0 = acc[0][0]; keep1 = acc[0][1]; }
        LAS f32x4* xs = (LAS f32x4*)(lds + ((wid & 3) * 2 + wk) * 8192) + lz;
#pragma unroll
        for (int g = 0; g < 4; ++g) {
            xs[g * 64] = (f32x4){give0[4 * g], give0[4 * g + 1], give0[4 * g + 2], give0[4 * g + 3]};
            xs[(4 + g) * 64] = (f32x4){give1[4 * g], give1[4 * g + 1], give1[4 * g + 2], give1[4 * g + 3]};
        }
        asm volatile("s_waitcnt lgkmcnt(0)\n\ts_barrier" ::: "memory");
        const LAS f32x4* xr = (const LAS f32x4*)(lds + ((wid & 3) * 2 + (1 - wk)) * 8192) + lz;
        const __amdgpu_buffer_rsrc_t rsP = __builtin_amdgcn_make_buffer_rsrc(P, 0, 8 << 20, 0x00020000);
        const unsigned pd = ((unsigned)(((ks * 16 + pm) * 8 + pn) * 8 + (wm * 2 + wk) * 2 + wn) * 256 + lz) * 16;
#pragma unroll
        for (int gp = 0; gp < 2; ++gp) {
            const int g = 2 * gp;
            const f32x4 a0 = (f32x4){keep0[4 * g], keep0[4 * g + 1], keep0[4 * g + 2], keep0[4 * g + 3]} + xr[g * 64];
            const f32x4 a1 = (f32x4){keep0[4 * g + 4], keep0[4 * g + 5], keep0[4 * g + 6], keep0[4 * g + 7]} + xr[(g + 1) * 64];
            const f32x4 b0 = (f32x4){keep1[4 * g], keep1[4 * g + 1], keep1[4 * g + 2], keep1[4 * g + 3]} + xr[(4 + g) * 64];
            const f32x4 b1 = (f32x4){keep1[4 * g + 4], keep1[4 * g + 5], keep1[4 * g + 6], keep1[4 * g + 7]} + xr[(4 + g + 1) * 64];
            const u32x4 wa = {cvtpk(a0[0], a0[1]), cvtpk(a0[2], a0[3]), cvtpk(a1[0], a1[1]), cvtpk(a1[2], a1[3])};
            const u32x4 wb = {cvtpk(b0[0], b0[1]), cvtpk(b0[2], b0[3]), cvtpk(b1[0], b1[1]), cvtpk(b1[2], b1[3])};
            __builtin_amdgcn_raw_buffer_store_b128(wa, rsP, pd + gp * 1024, 0, 16);
            __builtin_amdgcn_raw_buffer_store_b128(wb, rsP, pd + (2 + gp) * 1024, 0, 16);
        }
        asm volatile("s_waitcnt vmcnt(0)\n\ts_barrier" ::: "memory");
        if (threadIdx.x == 0) __hip_atomic_fetch_add(flags + (pm * 8 + pn) * 32, 1u, __ATOMIC_RELAXED, __HIP_MEMORY_SCOPE_AGENT);
        (void)rr; (void)hh;
    }
}

#define UNP(a, b, i) ((f32x4){__uint_as_float(a[i] << 16) + __uint_as_float(b[i] << 16), __uint_as_float(a[i] & 0xffff0000u) + __uint_as_float(b[i] & 0xffff0000u), \
                              __uint_as_float(a[(i) + 1] << 16) + __uint_as_float(b[(i) + 1] << 16), __uint_as_float(a[(i) + 1] & 0xffff0000u) + __uint_as_float(b[(i) + 1] & 0xffff0000u)})

__device__ __forceinline__ void wo_role(const u16* __restrict__ H, const u16* __restrict__ wd, const u16* __restrict__ Ob, const u16* __restrict__ wo, const float* __restrict__ P, const float* __restrict__ G, u16* __restrict__ out,
                                        unsigned* __restrict__ flags, unsigned* __restrict__ cnt, char* lds, int bid, unsigned long long* stamps)
{
    int tid = threadIdx.x; asm volatile("" : "+v"(tid));
    const int lane = tid & 63;
    const int wid = __builtin_amdgcn_readfirstlane(tid >> 6);
    const int wm = wid & 1, wn = (wid >> 1) & 1, wk = wid >> 2;
    const int xcd = bid & 7, idx = bid >> 3;
    const int rt = 8 * (xcd >> 1) + (idx & 7), pn = 4 * (xcd & 1) + (idx >> 3);
    const unsigned lds0 = (unsigned)(uintptr_t)lds;
    unsigned long long tw0 = 0ull, tw1, tw2, tw3;
    const int sr = lane >> 3, spos = lane & 7;
    const unsigned voffB = ((sr & 3) + 16 * (sr >> 2)) * 2048 + ((spos ^ (((sr >> 1) & 1) << 2)) * 16);
    const unsigned voffA = lane * 16;
    const char* pa = (const char*)H + ((size_t)(rt * 4 + (wid & 3)) * 112 + (112 - EXH) + (wid >> 2)) * 1024;
    const char* pb = (const char*)wd + (size_t)((112 - EXH) * 32 + 4 * (wid & 3)) * 2048 + pn * 256 + (wid >> 2) * 128;
    const char* paO = (const char*)Ob + ((size_t)(rt * 4 + (wid & 3)) * 32 + (wid >> 2)) * 1024;
    const char* pbO = (const char*)wo + (size_t)(4 * (wid & 3)) * 2048 + pn * 256 + (wid >> 2) * 128;
    int left = EXH / 2, sl = 0, sc = 0;
    const unsigned dA = lds0 + (wid >> 2) * 12288 + (wid & 3) * 1024, dB = lds0 + 4096 + wid * 1024;
#define K5_ISSUE() do { const unsigned so_ = sl * 24576; glds16s(pa, voffA, dA + so_); glds16s(pb, voffB, dB + so_); glds16s(pb + 32 * 2048, voffB, dB + so_ + 12288); \
                        pa += 2048; pb += (size_t)64 * 2048; sl = (sl == 2) ? 0 : sl + 1; --left; pa = (left == 0) ? paO : pa; pb = (left == 0) ? pbO : pb; } while (0)
    unsigned touch = 0;
    K5_ISSUE(); K5_ISSUE(); K5_ISSUE();
    const int r32 = lane & 31, hi = lane >> 5, fr = lane & 15;
    const unsigned aX0 = lds0 + wk * 12288 + (wm * 2 + (r32 >> 4)) * 1024 + fr * 64 + ((hi ^ ((-(fr >> 2)) & 3)) * 16);
    const int tq = (lane & 15) >> 2, tp = lane & 3;
    const unsigned aW0 = lds0 + wk * 12288 + 4096 + wn * 4096 + (8 * hi + tq) * 128 + (((tq >> 1) & 1) << 6) + ((lane >> 4) & 1) * 32 + tp * 8;
    f32x16 acc[2] = {};
#define K5_COMP() do { const unsigned so = sc * 24576; \
        const bf16x8 x0 = ldsb128(LP(aX0 + so)), x1 = ldsb128(LP((aX0 + so) ^ 32)); \
        const s16x4 w0 = vtr(LP(aW0 + so)), w1 = vtr(LP(aW0 + so) + 512), w2 = vtr(LP((aW0 + so) ^ 64)), w3 = vtr(LP((aW0 + so) ^ 64) + 512); \
        const s16x4 w4 = vtr(LP(aW0 + so) + 2048), w5 = vtr(LP(aW0 + so) + 2560), w6 = vtr(LP((aW0 + so) ^ 64) + 2048), w7 = vtr(LP((aW0 + so) ^ 64) + 2560); \
        acc[0] = MFMA32(cat8(w0, w1), x0, acc[0]); acc[1] = MFMA32(cat8(w2, w3), x0, acc[1]); acc[0] = MFMA32(cat8(w4, w5), x1, acc[0]); acc[1] = MFMA32(cat8(w6, w7), x1, acc[1]); \
        sc = (sc == 2) ? 0 : sc + 1; } while (0)
#define K5_RUN(n_) do { _Pragma("nounroll") for (int s2 = 0; s2 < (n_); ++s2) { asm volatile("" : "+s"(sl), "+s"(sc), "+s"(left)); VW(3); K5_ISSUE(); K5_COMP(); } } while (0)
    VW(6); K5_COMP();
    K5_RUN(EXH / 2 - 3);
    if (wid == 0) {
        unsigned spins = 0;
        while (__builtin_amdgcn_readfirstlane(__hip_atomic_load(cnt + (rt >> 2) * 32, __ATOMIC_RELAXED, __HIP_MEMORY_SCOPE_AGENT)) < 32u) { __builtin_amdgcn_s_sleep(2); if (++spins > (1u << 22)) break; }
        __builtin_amdgcn_fence(__ATOMIC_ACQUIRE, "agent");
        asm volatile("s_waitcnt vmcnt(0)" ::: "memory");
    }
    tw1 = 0ull;
    asm volatile("s_waitcnt lgkmcnt(0)\n\ts_barrier" ::: "memory");
    K5_RUN(2);
    {
        int lz0 = threadIdx.x & 31; asm volatile("" : "+v"(lz0));
        const float gm = 1.0f - G[rt * 64 + wm * 32 + lz0];
        acc[0] *= gm; acc[1] *= gm;
    }
    K5_RUN(14);
    VW(3); K5_COMP();
    VW(0); K5_COMP();
    asm volatile("" :: "v"(touch));
    tw2 = 0ull;
    if (wid == 0) {
        unsigned spins = 0;
        while (__builtin_amdgcn_readfirstlane(__hip_atomic_load(flags + ((rt >> 1) * 8 + pn) * 32, __ATOMIC_RELAXED, __HIP_MEMORY_SCOPE_AGENT)) < 2u) { __builtin_amdgcn_s_sleep(1); if (++spins > (1u << 22)) break; }
    }
    int lz = threadIdx.x & 63; asm volatile("" : "+v"(lz));
    const int rr = lz & 31, hh = lz >> 5;
    f32x16 give, keep; if (wk) { give = acc[0]; keep = acc[1]; } else { give = acc[1]; keep = acc[0]; }
    asm volatile("s_waitcnt vmcnt(0) lgkmcnt(0)\n\ts_barrier" ::: "memory");
    tw3 = 0ull;
    const __amdgpu_buffer_rsrc_t rsP = __builtin_amdgcn_make_buffer_rsrc((void*)P, 0, 8 << 20, 0x00020000);
    const unsigned blk = (unsigned)(((rt >> 1) * 8 + pn) * 8 + ((rt & 1) * 2 + wm) * 2 + wn);
    const unsigned p0 = (blk * 256 + wk * 128 + lz) * 16, p1 = p0 + 16 * 8 * 8 * 256 * 16;
    u32x4 pv[4];
    pv[0] = __builtin_amdgcn_raw_buffer_load_b128(rsP, p0, 0, 16); pv[1] = __builtin_amdgcn_raw_buffer_load_b128(rsP, p0 + 1024, 0, 16);
    pv[2] = __builtin_amdgcn_raw_buffer_load_b128(rsP, p1, 0, 16); pv[3] = __builtin_amdgcn_raw_buffer_load_b128(rsP, p1 + 1024, 0, 16);
    const float gm = 1.0f - G[rt * 64 + wm * 32 + rr];
    LAS f32x4* xs = (LAS f32x4*)(lds + ((wid & 3) * 2 + wk) * 4096) + lz;
#pragma unroll
    for (int g = 0; g < 4; ++g) xs[g * 64] = (f32x4){give[4 * g], give[4 * g + 1], give[4 * g + 2], give[4 * g + 3]};
    asm volatile("s_waitcnt lgkmcnt(0)\n\ts_barrier" ::: "memory");
    const LAS f32x4* xr = (const LAS f32x4*)(lds + ((wid & 3) * 2 + (1 - wk)) * 4096) + lz;
    const int row = rt * 64 + wm * 32 + rr;
#pragma unroll
    for (int g = 0; g < 4; g += 2) {
        const f32x4 v0 = (f32x4){keep[4 * g], keep[4 * g + 1], keep[4 * g + 2], keep[4 * g + 3]} + xr[g * 64] + UNP(pv[g >> 1], pv[2 + (g >> 1)], 0) * gm;
        const f32x4 v1 = (f32x4){keep[4 * g + 4], keep[4 * g + 5], keep[4 * g + 6], keep[4 * g + 7]} + xr[(g + 1) * 64] + UNP(pv[g >> 1], pv[2 + (g >> 1)], 2) * gm;
        const unsigned ax = cvtpk(v0[0], v0[1]), ay = cvtpk(v0[2], v0[3]), bx = cvtpk(v1[0], v1[1]), by = cvtpk(v1[2], v1[3]);
        auto rx = __builtin_amdgcn_permlane32_swap(ax, bx, false, false); auto ry = __builtin_amdgcn_permlane32_swap(ay, by, false, false);
        const u32x4 w = {rx[0], ry[0], rx[1], ry[1]};
        const int col = pn * 128 + wn * 64 + wk * 32 + (g + hh) * 8;
        *(u32x4*)(out + (size_t)row * 1024 + col) = w;
    }
}

__global__ __launch_bounds__(512, 4) void k23_fused(const u16* __restrict__ QKV, const float* __restrict__ KMAX, const float* __restrict__ G, u16* __restrict__ O,
                                                    const u16* __restrict__ H, const u16* __restrict__ wd, const u16* __restrict__ wo, u16* __restrict__ out, float* __restrict__ P,
                                                    unsigned* __restrict__ flags, unsigned long long* stamps)
{
    extern __shared__ __attribute__((aligned(16))) char lds[];
    const int bid = blockIdx.x;
    unsigned* cnt = flags + 256 * 32;
    if (bid < 256) { attn_role(QKV, KMAX, G, O, cnt, lds, bid, stamps); wo_role(H, wd, O, wo, P, G, out, flags, cnt, lds, bid, stamps); }
    else gemm_role(H, wd, P, flags, lds, bid, stamps);
}

extern "C" void kernel_launch(void* const* d_in, const int* in_sizes, int n_in, void* d_out, int out_size, void* d_ws, size_t ws_size, hipStream_t stream)
{
    const u16* x = (const u16*)d_in[0]; const u16* wqkv = (const u16*)d_in[1]; const u16* wo = (const u16*)d_in[2];
    const u16* wg = (const u16*)d_in[4]; const u16* wu = (const u16*)d_in[5]; const u16* wd = (const u16*)d_in[6]; const u16* gw = (const u16*)d_in[7];
    char* ws = (char*)d_ws;
    u16* QKV = (u16*)ws;
    u16* H = (u16*)(ws + (16u << 20));
    u16* Ob = (u16*)(ws + (32u << 20));
    float* P = (float*)(ws + (40u << 20));
    float* G = (float*)(ws + (48u << 20));
    float* KM = (float*)(ws + (48u << 20) + 65536);
    unsigned* flags = (unsigned*)(ws + (48u << 20) + 131072);
    hipFuncSetAttribute((const void*)k1_gemm, hipFuncAttributeMaxDynamicSharedMemorySize, K1_LDS);
    hipFuncSetAttribute((const void*)k23_fused, hipFuncAttributeMaxDynamicSharedMemorySize, K23_LDS);
    hipLaunchKernelGGL(k1_gemm, dim3(256), dim3(512), K1_LDS, stream, x, wqkv, wg, wu, gw, QKV, H, G, KM, flags, wd, wo, (unsigned long long*)nullptr);
    hipLaunchKernelGGL(k23_fused, dim3(512), dim3(512), K23_LDS, stream, QKV, KM, G, Ob, H, wd, wo, (u16*)d_out, P, flags, (unsigned long long*)nullptr);
}
```

```cpp
#define K1_AUX 2
#define EXH 8
#define NO_TOUCH
#define ATT_PRIO 1
#define GELU_PK
#include <hip/hip_runtime.h>
#include <stdint.h>
typedef unsigned short u16;
typedef short bf16x8 __attribute__((ext_vector_type(8)));
typedef short s16x4 __attribute__((ext_vector_type(4)));
typedef float f32x4 __attribute__((ext_vector_type(4)));
typedef float f32x16 __attribute__((ext_vector_type(16)));
typedef unsigned u32x4 __attribute__((ext_vector_type(4)));
#define LAS __attribute__((address_space(3)))
typedef LAS const char* lds_cptr;
typedef short v4i16_t __attribute__((ext_vector_type(4)));
#define SBAR() __builtin_amdgcn_sched_barrier(0)

constexpr int S_ = 2048, D_ = 1024, F_ = 3584, NH_ = 32;
constexpr float QSCALE = 0.17677669529663687f * 1.4426950408889634f;

typedef __bf16 bf16x2_t __attribute__((ext_vector_type(2)));
typedef float f32x2_t __attribute__((ext_vector_type(2)));
__device__ __forceinline__ unsigned cvtpk(float lo, float hi) { const f32x2_t v = {lo, hi}; return __builtin_bit_cast(unsigned, __builtin_convertvector(v, bf16x2_t)); }
__device__ __forceinline__ void glds16s(const void* sbase, unsigned voff, unsigned lds_base) {
    asm volatile("s_mov_b32 m0, %2\n\ts_nop 0\n\tglobal_load_lds_dwordx4 %0, %1"
                 :: "v"(voff), "s"(sbase), "s"(lds_base) : "memory");
}
__device__ __forceinline__ s16x4 vtr(lds_cptr p) { return __builtin_bit_cast(s16x4, __builtin_amdgcn_ds_read_tr16_b64_v4i16((LAS v4i16_t*)p)); }
__device__ __forceinline__ bf16x8 cat8(s16x4 a, s16x4 b) { return (bf16x8){a[0], a[1], a[2], a[3], b[0], b[1], b[2], b[3]}; }
__device__ __forceinline__ bf16x8 ldsb128(lds_cptr p) { return *(const LAS bf16x8*)p; }

__device__ __forceinline__ lds_cptr LP(unsigned a) { return (lds_cptr)(uintptr_t)a; }
template <int X> __device__ __forceinline__ float swz_xor(float v) { return __int_as_float(__builtin_amdgcn_ds_swizzle(__float_as_int(v), (X << 10) | 0x1f)); }
__device__ __forceinline__ float add32(float v) { auto r = __builtin_amdgcn_permlane32_swap(__float_as_uint(v), __float_as_uint(v), false, false); return __uint_as_float(r[0]) + __uint_as_float(r[1]); }
__device__ __forceinline__ float max32(float v) { auto r = __builtin_amdgcn_permlane32_swap(__float_as_uint(v), __float_as_uint(v), false, false); return fmaxf(__uint_as_float(r[0]), __uint_as_float(r[1])); }
constexpr int K1_STAGE = 73728, K1_LDS = 2 * K1_STAGE;

__device__ __forceinline__ float gelu_mul(float g, float u) {
    const float t = g * g;
    const float p = __builtin_fmaf(t, -0.1029432397f, -2.302208198f);
    const float e = __builtin_amdgcn_exp2f(g * p);
    const float r = __builtin_amdgcn_rcpf(1.0f + e);
    return g * u * r;
}

__device__ __forceinline__ unsigned gelu_mul2(float g0, float g1, float u0, float u1) {
    const f32x2_t g = {g0, g1}, u = {u0, u1};
    const f32x2_t t = g * g;
    const f32x2_t p = t * -0.1029432397f + -2.302208198f;
    const f32x2_t z = g * p;
    f32x2_t e; e[0] = __builtin_amdgcn_exp2f(z[0]); e[1] = __builtin_amdgcn_exp2f(z[1]);
    const f32x2_t d = e + 1.0f;
    f32x2_t r; r[0] = __builtin_amdgcn_rcpf(d[0]); r[1] = __builtin_amdgcn_rcpf(d[1]);
    const f32x2_t h = (g * u) * r;
    return __builtin_bit_cast(unsigned, __builtin_convertvector(h, bf16x2_t));
}

__global__ __launch_bounds__(512, 2) void k1_gemm(const u16* __restrict__ x, const u16* __restrict__ wqkv, const u16* __restrict__ wg, const u16* __restrict__ wu,
                                                  const u16* __restrict__ gw, u16* __restrict__ QKV, u16* __restrict__ H, float* __restrict__ G, float* __restrict__ KMAX, unsigned* __restrict__ flags, const u16* __restrict__ wd, const u16* __restrict__ wo, unsigned long long* stamps)
{
    extern __shared__ __attribute__((aligned(16))) char lds[];
    const int tid = threadIdx.x, lane = tid & 63;
    const int wid = __builtin_amdgcn_readfirstlane(tid >> 6);
    const int wm = wid & 3, wn = wid >> 2;
    const int fr = lane & 15, fq = lane >> 4;
    const int bid = blockIdx.x, xcd = bid & 7, idx = bid >> 3, pm = idx & 7, pn = xcd * 4 + (idx >> 3);
    const bool typeB = pn >= 24;
    const unsigned lds0 = (unsigned)(uintptr_t)lds;
    unsigned long long ts0=0ull, tr0=0ull;

    if (bid == 0) { for (int i = tid; i < 264 * 32; i += 512) flags[i] = 0u; }
    const char* cb0; const char* cb1; const char* cb2; const char* cb3; const char* cb4;
    unsigned ldmid;
    if (!typeB) { cb0 = (const char*)(wg + 128 * pn); cb1 = (const char*)(wu + 128 * pn); cb2 = cb0 + 128; cb3 = cb1 + 128; cb4 = (const char*)(wqkv + 64 * pn); ldmid = F_ * 2; }
    else { const int i = pn - 24; cb0 = (const char*)(wg + 3072 + 64 * i); cb1 = (const char*)(wu + 3072 + 64 * i); cb2 = (const char*)(wqkv + 64 * (24 + 3 * i)); cb3 = cb2 + 128; cb4 = cb2 + 256; ldmid = 3072 * 2; }
    cb0 += (size_t)(8 * wid) * (F_ * 2); cb1 += (size_t)(8 * wid) * (F_ * 2); cb2 += (size_t)(8 * wid) * ldmid; cb3 += (size_t)(8 * wid) * ldmid; cb4 += (size_t)(8 * wid) * (3072 * 2);
    const char* ca = (const char*)x + (size_t)(256 * pm + 8 * wid) * 2048;
    const int sr = lane >> 3, spos = lane & 7;
    const unsigned fB = ((sr >> 1) & 1) | ((wid & 1) << 1);
    const unsigned colB = (((spos >> 1) ^ fB) * 32) + (spos & 1) * 16;
    const unsigned voffG = sr * (F_ * 2) + colB, voffM = sr * ldmid + colB, voffQ = sr * (3072 * 2) + colB;
    const unsigned voffA = sr * 2048 + ((spos ^ (4 * (wid & 1) + (sr >> 1))) * 16);
    const unsigned ldsw = lds0 + wid * 1024;
    float gsig;

#define K1_PIECE(i, sb) do { \
        if ((i) < 4) glds16s(ca + (size_t)(i) * 131072, voffA, (sb) + (i) * 8192); \
        else if ((i) == 4) glds16s(cb0, voffG, (sb) + 32768); \
        else if ((i) == 5) glds16s(cb2, voffM, (sb) + 32768 + 16384); \
        else if ((i) == 6) glds16s(cb1, voffG, (sb) + 32768 + 8192); \
        else if ((i) == 7) glds16s(cb3, voffM, (sb) + 32768 + 24576); \
        else glds16s(cb4, voffQ, (sb) + 32768 + 32768); } while (0)
#define K1_ADV1() do { ca += 128; cb0 += 64 * F_ * 2; cb2 += (size_t)64 * ldmid; } while (0)
#define K1_ADV2() do { cb1 += 64 * F_ * 2; cb3 += (size_t)64 * ldmid; cb4 += 64 * 3072 * 2; } while (0)

    const int grow = 256 * pm + 8 * pn + wid;
    u32x4 gx0, gx1, gg0, gg1;
    {
        const char* xr = (const char*)(x + (size_t)grow * D_);
        const unsigned go = lane * 32;
        asm volatile("global_load_dwordx4 %0, %4, %5\n\tglobal_load_dwordx4 %1, %4, %5 offset:16\n\tglobal_load_dwordx4 %2, %4, %6\n\tglobal_load_dwordx4 %3, %4, %6 offset:16"
                     : "=&v"(gx0), "=&v"(gx1), "=&v"(gg0), "=&v"(gg1) : "v"(go), "s"(xr), "s"(gw) : "memory");
    }
#pragma unroll
    for (int i = 0; i < 9; ++i) K1_PIECE(i, ldsw);
    K1_ADV1(); K1_ADV2();
#pragma unroll
    for (int i = 0; i < 6; ++i) K1_PIECE(i, ldsw + K1_STAGE);
    K1_ADV1();
#define GWAIT 15
    unsigned long long tsa=0ull;
#define STR2(x) #x
#define STR(x) STR2(x)
    asm volatile("s_waitcnt vmcnt(" STR(GWAIT) ")" : "+v"(gx0), "+v"(gx1), "+v"(gg0), "+v"(gg1) :: "memory");
    {
        float s = 0.f;
#pragma unroll
        for (int j = 0; j < 4; ++j) {
            s += __uint_as_float(gx0[j] << 16) * __uint_as_float(gg0[j] << 16) + __uint_as_float(gx0[j] & 0xffff0000u) * __uint_as_float(gg0[j] & 0xffff0000u);
            s += __uint_as_float(gx1[j] << 16) * __uint_as_float(gg1[j] << 16) + __uint_as_float(gx1[j] & 0xffff0000u) * __uint_as_float(gg1[j] & 0xffff0000u);
        }
        s += swz_xor<1>(s); s += swz_xor<2>(s); s += swz_xor<4>(s); s += swz_xor<8>(s); s += swz_xor<16>(s); s = add32(s);
        gsig = 1.0f / (1.0f + __expf(-s));
    }

    unsigned long long tsb=0ull;
    unsigned aA0 = lds0 + (64 * wm + fr) * 128 + (((0 + fq) ^ (fr >> 1)) * 16);
    unsigned aA1 = lds0 + (64 * wm + fr) * 128 + (((4 + fq) ^ (fr >> 1)) * 16);
    const int tq = (lane & 15) >> 2, tp = lane & 3;
    const unsigned fR = ((tq >> 1) & 1) | ((fq & 1) << 1);
    const unsigned bbase = lds0 + 32768 + (8 * fq + tq) * 128 + 8 * tp;
    unsigned aB0 = bbase + 2 * wn * 8192 + ((0 ^ fR) << 5);
    unsigned aB1 = bbase + 2 * wn * 8192 + ((1 ^ fR) << 5);
    unsigned aB2 = bbase + 2 * wn * 8192 + ((2 ^ fR) << 5);
    unsigned aB3 = bbase + 2 * wn * 8192 + ((3 ^ fR) << 5);
    unsigned aB4a = bbase + 4 * 8192 + (((2 * wn) ^ fR) << 5);
    unsigned aB4b = bbase + 4 * 8192 + (((2 * wn + 1) ^ fR) << 5);

    f32x4 acc[4][10];
#pragma unroll
    for (int m = 0; m < 4; ++m)
#pragma unroll
        for (int s = 0; s < 10; ++s) acc[m][s] = (f32x4){0.f, 0.f, 0.f, 0.f};
    bf16x8 fa0[4], fa1[4];
    s16x4 bx[4], by[4];

#define MF(fb, h, fa, m, ti) acc[m][(ti) + (h)] = __builtin_amdgcn_mfma_f32_16x16x32_bf16(cat8(fb[2 * (h)], fb[2 * (h) + 1]), fa[m], acc[m][(ti) + (h)], 0, 0, 0)
#define RB(dst, j, aa, ab, off) dst[j] = vtr(LP(((j) < 2) ? (aa) : (ab)) + (off) + ((j) & 1) * 512)
#define RA(dst, m, a) dst[m] = ldsb128(LP(a) + (m) * 2048)
#define SUBP(cur, fa, ti, nxt, aa, ab, off, AR, GL) do { \
        MF(cur, 0, fa, 0, ti); SBAR(); RB(nxt, 0, aa, ab, off); SBAR(); \
        MF(cur, 1, fa, 0, ti); SBAR(); RB(nxt, 1, aa, ab, off); SBAR(); \
        MF(cur, 0, fa, 1, ti); SBAR(); RB(nxt, 2, aa, ab, off); SBAR(); \
        MF(cur, 1, fa, 1, ti); SBAR(); RB(nxt, 3, aa, ab, off); SBAR(); \
        MF(cur, 0, fa, 2, ti); SBAR(); AR(0); GL; SBAR(); \
        MF(cur, 1, fa, 2, ti); SBAR(); AR(1); SBAR(); \
        MF(cur, 0, fa, 3, ti); SBAR(); AR(2); SBAR(); \
        MF(cur, 1, fa, 3, ti); SBAR(); AR(3); SBAR(); } while (0)
#define NOA(m) do { } while (0)
#define RA0(m) RA(fa0, m, aA0)
#define RA1(m) RA(fa1, m, aA1)
#define NOG do { } while (0)
#define WBAR(n) asm volatile("s_waitcnt vmcnt(" #n ") lgkmcnt(0)\n\ts_barrier" ::: "memory")

    unsigned warm0 = 0;
    const int wgi = bid * 8 + wid;
    const char* wp = wgi < 1275 ? (const char*)wd + min(wgi * 5760, 7340032 - 5760) : (const char*)wo + min((wgi - 1275) * 5760, 2097152 - 5760);
    const unsigned wvo = (lane % 6) * 64;
    asm volatile("global_load_dword %0, %1, %2" : "+v"(warm0) : "v"(wvo), "s"(wp) : "memory"); wp += 384;
    K1_PIECE(6, ldsw + K1_STAGE);
    asm volatile("s_waitcnt vmcnt(11)\n\ts_barrier" ::: "memory");
    if (lane == 0) G[grow] = gsig;
#pragma unroll
    for (int j = 0; j < 4; ++j) RB(bx, j, aB0, aB1, 0);
#pragma unroll
    for (int m = 0; m < 4; ++m) RA0(m);
    int delta = K1_STAGE;
    unsigned long long ts1=0ull; tsa=0ull;

#define WBARM() WBAR(10)
#define VADD(a) asm volatile("v_add_u32 %0, %1, %0" : "+v"(a) : "s"(delta))
#define STEP(MAIN) do { \
        SUBP(bx, fa0, 0, by, aB0, aB1, 4096, RA1, if (MAIN || p2) K1_PIECE(7, ns)); \
        SUBP(by, fa1, 0, bx, aB2, aB3, 0, NOA, if (MAIN || p2) { K1_PIECE(8, ns); K1_ADV2(); }); \
        SUBP(bx, fa0, 2, by, aB2, aB3, 4096, NOA, NOG); \
        if (MAIN) WBARM(); else if (p2) WBAR(9); else WBAR(0); \
        SUBP(by, fa1, 2, bx, aB0, aB1, 8192, NOA, if (MAIN) K1_PIECE(0, cs)); \
        SUBP(bx, fa0, 4, by, aB0, aB1, 8192 + 4096, NOA, if (MAIN) K1_PIECE(1, cs)); \
        SUBP(by, fa1, 4, bx, aB2, aB3, 8192, NOA, if (MAIN) K1_PIECE(2, cs)); \
        SUBP(bx, fa0, 6, by, aB2, aB3, 8192 + 4096, NOA, if (MAIN) K1_PIECE(3, cs)); \
        SUBP(by, fa1, 6, bx, aB4a, aB4b, 0, NOA, if (MAIN) K1_PIECE(4, cs)); \
        SUBP(bx, fa0, 8, by, aB4a, aB4b, 4096, NOA, if (MAIN) { K1_PIECE(5, cs); K1_ADV1(); }); \
        VADD(aA0); VADD(aA1); VADD(aB0); VADD(aB1); VADD(aB2); VADD(aB3); \
        if (MAIN) WBARM(); else WBAR(3); \
        SUBP(by, fa1, 8, bx, aB0, aB1, 0, RA0, if (MAIN) K1_PIECE(6, cs)); \
        VADD(aB4a); VADD(aB4b); delta = -delta; { const unsigned tmp = cs; cs = ns; ns = tmp; } \
    } while (0)
    unsigned cs = ldsw, ns = ldsw + K1_STAGE;
#pragma nounroll
    for (int tt = 0; tt < 14; ++tt) {
        const bool p2 = true; STEP(true); asm volatile("" : "+s"(cs), "+s"(ns), "+s"(delta));
        asm volatile("global_load_dword %0, %1, %2" : "+v"(warm0) : "v"(wvo), "s"(wp) : "memory"); wp += 384;
    }
#pragma nounroll
    for (int tt = 0; tt < 2; ++tt) { int t = tt; asm volatile("" : "+s"(t)); const bool p2 = t == 0; STEP(false); asm volatile("" : "+s"(cs), "+s"(ns), "+s"(delta)); }
    unsigned long long ts2=0ull; tsb=0ull;

    const int row0 = 256 * pm + 64 * wm;
    const __amdgpu_buffer_rsrc_t rsH = __builtin_amdgcn_make_buffer_rsrc(H, 0, 2048 * 3584 * 2, 0x00020000), rsQ = __builtin_amdgcn_make_buffer_rsrc(QKV, 0, 96 * 2048 * 64, 0x00020000);
#define ST16(base, rs, off, w) __builtin_amdgcn_raw_buffer_store_b128(w, rs, off, 0, K1_AUX)
    const unsigned swz = ((fq ^ ((-(fr >> 2)) & 3)) * 16) + fr * 64;
    const bool gu = !typeB || wn == 0;
    if (gu) {
        const int hc0 = typeB ? (3072 + 64 * (pn - 24)) : (128 * pn + 64 * wn);
#pragma unroll
        for (int m = 0; m < 4; ++m) {
            const int mt = (row0 >> 4) + m;
#pragma unroll
            for (int i = 0; i < 2; ++i) {
                u32x4 w;
                { const f32x4 g = acc[m][2 * i], u = acc[m][4 + 2 * i];
                  w[0] = gelu_mul2(g[0], g[1], u[0], u[1]); w[1] = gelu_mul2(g[2], g[3], u[2], u[3]); }
                { const f32x4 g = acc[m][2 * i + 1], u = acc[m][4 + 2 * i + 1];
                  w[2] = gelu_mul2(g[0], g[1], u[0], u[1]); w[3] = gelu_mul2(g[2], g[3], u[2], u[3]); }
                ST16(H, rsH, ((unsigned)mt * 112 + (hc0 >> 5) + i) * 1024 + swz, w);
            }
        }
    }
    auto store_head = [&](int hb, const f32x4 (&ta)[4], const f32x4 (&tb)[4]) {
        const int which = hb >> 5;
        const float sc = which == 0 ? QSCALE : 1.0f;
        float kn = 0.f;
#pragma unroll
        for (int m = 0; m < 4; ++m) {
            const f32x4 a = ta[m] * sc, b = tb[m] * sc;
            u32x4 w; w[0] = cvtpk(a[0], a[1]); w[1] = cvtpk(a[2], a[3]); w[2] = cvtpk(b[0], b[1]); w[3] = cvtpk(b[2], b[3]);
            ST16(QKV, rsQ, ((unsigned)hb * 2048 + row0 + 16 * m + fr) * 64 + fq * 16, w);
            if (which == 1) {
                float n2 = a[0] * a[0] + a[1] * a[1] + a[2] * a[2] + a[3] * a[3] + b[0] * b[0] + b[1] * b[1] + b[2] * b[2] + b[3] * b[3];
                n2 += swz_xor<16>(n2); n2 = add32(n2);
                kn = fmaxf(kn, n2);
            }
        }
        if (which == 1) {
            kn = fmaxf(kn, swz_xor<8>(kn)); kn = fmaxf(kn, swz_xor<4>(kn)); kn = fmaxf(kn, swz_xor<2>(kn)); kn = fmaxf(kn, swz_xor<1>(kn));
            if (lane == 0) KMAX[(hb & 31) * 32 + pm * 4 + wm] = kn;
        }
    };
    {
        const int qc4 = typeB ? (24 + 3 * (pn - 24) + 2) : pn;
        const f32x4 ta[4] = {acc[0][8], acc[1][8], acc[2][8], acc[3][8]};
        const f32x4 tb[4] = {acc[0][9], acc[1][9], acc[2][9], acc[3][9]};
        store_head(2 * qc4 + wn, ta, tb);
    }
    if (!gu) {
        const int qc2 = 24 + 3 * (pn - 24);
#pragma unroll
        for (int c = 0; c < 2; ++c)
#pragma unroll
            for (int hh = 0; hh < 2; ++hh) {
                const f32x4 ta[4] = {acc[0][4 * c + 2 * hh], acc[1][4 * c + 2 * hh], acc[2][4 * c + 2 * hh], acc[3][4 * c + 2 * hh]};
                const f32x4 tb[4] = {acc[0][4 * c + 2 * hh + 1], acc[1][4 * c + 2 * hh + 1], acc[2][4 * c + 2 * hh + 1], acc[3][4 * c + 2 * hh + 1]};
                store_head(2 * (qc2 + c) + hh, ta, tb);
            }
    }
    asm volatile("" :: "v"(warm0));
}

#define MFMA32(a, b, c) __builtin_amdgcn_mfma_f32_32x32x16_bf16(a, b, c, 0, 0, 0)
#define MFMA16(a, b, c) __builtin_amdgcn_mfma_f32_16x16x32_bf16(a, b, c, 0, 0, 0)
constexpr int K2_SLOT = 16384, K2_NS = 4, K2_LDS = K2_SLOT * K2_NS;

__device__ __forceinline__ bf16x8 pack8(const f32x16& p, int b) {
    u32x4 w; w[0] = cvtpk(p[b], p[b + 1]); w[1] = cvtpk(p[b + 2], p[b + 3]); w[2] = cvtpk(p[b + 4], p[b + 5]); w[3] = cvtpk(p[b + 6], p[b + 7]);
    return __builtin_bit_cast(bf16x8, w);
}

#define K2_ISSUE(st) do { glds16s(Kh + (size_t)(st) * 8192, voffK, ldsw + ((st) & 3) * K2_SLOT); glds16s(Vh + (size_t)(st) * 8192, voffV, ldsw + ((st) & 3) * K2_SLOT + 8192); } while (0)
#define RDK(d0, d1, slot, i) do { d0 = ldsb128(LP(aK0 + (slot)) + (i) * 2048); d1 = ldsb128(LP(aK1 + (slot)) + (i) * 2048); } while (0)
#define QK(S, k0, k1) do { S = MFMA32(k0, qf0, negm); S = MFMA32(k1, qf1, S); } while (0)
#define PIN(x) asm volatile("" : "+v"(x))
#define PKA(a, S, i) a += (f32x2_t){S[i], S[(i) + 1]}
#define SUM0 do { PKA(ls0, SCUR, 0); PKA(ls1, SCUR, 2); PKA(ls0, SCUR, 4); PKA(ls1, SCUR, 6); PIN(ls0); PIN(ls1); } while (0)
#define SUM1 do { PKA(ls0, SCUR, 8); PKA(ls1, SCUR, 10); PKA(ls0, SCUR, 12); PKA(ls1, SCUR, 14); PIN(ls0); PIN(ls1); } while (0)
#define SUM0 lsum = MFMA16(sel, p0, lsum)
#define SUM1 lsum2 = MFMA16(sel, p1, lsum2)
#define EX4(S, b) do { S[b] = __builtin_amdgcn_exp2f(S[b]); S[b + 1] = __builtin_amdgcn_exp2f(S[b + 1]); S[b + 2] = __builtin_amdgcn_exp2f(S[b + 2]); S[b + 3] = __builtin_amdgcn_exp2f(S[b + 3]); } while (0)
#define RDV(d, slot, i) do { d[0] = vtr(LP(aV + (slot)) + (i) * 2048); d[1] = vtr(LP(aV + (slot)) + (i) * 2048 + 512); d[2] = vtr(LP(aV + (slot)) + (i) * 2048 + 1024); d[3] = vtr(LP(aV + (slot)) + (i) * 2048 + 1536); } while (0)
#define ITER(SC, SN, k0, k1, n0, n1, vc, vn, kslot, ki, vslot, vi) do { \
        f32x16& SCUR = SC; (void)SCUR; \
        SN = MFMA32(k0, qf0, negm); SBAR(); \
        EX4(SC, 0); PIN(SC); SBAR(); \
        SN = MFMA32(k1, qf1, SN); SBAR(); \
        RDK(n0, n1, kslot, ki); SBAR(); \
        EX4(SC, 4); const bf16x8 p0 = pack8(SC, 0); SBAR(); \
        o = MFMA32(cat8(vc[0], vc[1]), p0, o); SBAR(); \
        EX4(SC, 8); PIN(SC); SBAR(); \
        SUM0; SBAR(); \
        EX4(SC, 12); const bf16x8 p1 = pack8(SC, 8); SBAR(); \
        o = MFMA32(cat8(vc[2], vc[3]), p1, o); SBAR(); \
        RDV(vn, vslot, vi); SBAR(); \
        SUM1; SBAR(); \
    } while (0)
#define WB2(n) asm volatile("s_waitcnt vmcnt(" #n ") lgkmcnt(0)\n\ts_barrier" ::: "memory")
constexpr int K23_LDS = 81920;
#define GEMM_PRIO 0

__device__ __forceinline__ void attn_role(const u16* __restrict__ QKV, const float* __restrict__ KMAX, const float* __restrict__ G, u16* __restrict__ O, unsigned* __restrict__ cnt, char* lds, int bid, unsigned long long* stamps)
{
    const int tid = threadIdx.x, lane = tid & 63;
    const int wid = __builtin_amdgcn_readfirstlane(tid >> 6);
    const int r32 = lane & 31, hi = lane >> 5;
    const int xcd = bid & 7, idx = bid >> 3, head = 4 * xcd + (idx >> 3), qb = idx & 7;
    const unsigned lds0 = (unsigned)(uintptr_t)lds;
    unsigned long long ts0 = 0ull, tr0 = 0ull;
    if (ATT_PRIO) __builtin_amdgcn_s_setprio(ATT_PRIO);
    const char* Qh = (const char*)QKV + (size_t)head * 131072;
    const char* Kh = (const char*)QKV + (size_t)(32 + head) * 131072 + wid * 1024;
    const char* Vh = (const char*)QKV + (size_t)(64 + head) * 131072 + wid * 1024;
    const unsigned voffK = (lane >> 2) * 64 + (((lane & 3) ^ ((lane >> 4) & 3)) * 16), voffV = lane * 16;
    const unsigned ldsw = lds0 + wid * 1024;
    K2_ISSUE(0); K2_ISSUE(1);
    const int q0 = qb * 256 + wid * 32;
    const bf16x8 qf0 = *(const bf16x8*)(Qh + (size_t)(q0 + r32) * 64 + hi * 16);
    const bf16x8 qf1 = *(const bf16x8*)(Qh + (size_t)(q0 + r32) * 64 + 32 + hi * 16);
    const float kmx = KMAX[head * 32 + r32];
    float gq = G[q0 + r32];
    float qn2 = 0.f;
#pragma unroll
    for (int j = 0; j < 8; ++j) { const float a = __uint_as_float((unsigned)(u16)qf0[j] << 16), b = __uint_as_float((unsigned)(u16)qf1[j] << 16); qn2 += a * a + b * b; }
    qn2 = add32(qn2);
    float km2 = kmx; km2 = fmaxf(km2, swz_xor<1>(km2)); km2 = fmaxf(km2, swz_xor<2>(km2)); km2 = fmaxf(km2, swz_xor<4>(km2)); km2 = fmaxf(km2, swz_xor<8>(km2)); km2 = fmaxf(km2, swz_xor<16>(km2));
    const float mb = sqrtf(qn2 * km2) * 1.01f;
    f32x16 negm;
#pragma unroll
    for (int r = 0; r < 16; ++r) negm[r] = -mb;
    asm volatile("" : "+v"(negm), "+v"(gq));
    bf16x8 sel;
    { const short one = ((lane & 15) == ((lane >> 4) & 1)) ? (short)0x3F80 : (short)0;
#pragma unroll
      for (int j = 0; j < 8; ++j) sel[j] = one; }
    const unsigned aK0 = lds0 + r32 * 64 + (((0 + hi) ^ ((r32 >> 2) & 3)) * 16);
    const unsigned aK1 = lds0 + r32 * 64 + (((2 + hi) ^ ((r32 >> 2) & 3)) * 16);
    const unsigned aV = lds0 + 8192 + (4 * hi + ((lane & 15) >> 2)) * 64 + ((lane >> 4) & 1) * 32 + (lane & 3) * 8;
    f32x16 o = {}; f32x4 lsum = {0.f, 0.f, 0.f, 0.f}, lsum2 = {0.f, 0.f, 0.f, 0.f}; f32x2_t ls0 = {0.f, 0.f}, ls1 = {0.f, 0.f};
    f32x16 sA, sB;
    bf16x8 kfa0, kfa1, kfb0, kfb1;
    s16x4 va[4], vb[4];
    WB2(2);
    K2_ISSUE(2);
    RDK(kfa0, kfa1, 0, 0); RDK(kfb0, kfb1, 0, 1); RDV(va, 0, 0);
    QK(sA, kfa0, kfa1);
    unsigned long long ts1 = 0ull;
#define ATT_STAGE(MAIN) do { \
        const unsigned cur = (stv & 3) * K2_SLOT, nxt = ((stv + 1) & 3) * K2_SLOT; \
        ITER(sA, sB, kfb0, kfb1, kfa0, kfa1, va, vb, cur, 2, cur, 1); \
        ITER(sB, sA, kfa0, kfa1, kfb0, kfb1, vb, va, cur, 3, cur, 2); \
        if (MAIN) { WB2(2); K2_ISSUE(stv + 3); } else WB2(0); \
        ITER(sA, sB, kfb0, kfb1, kfa0, kfa1, va, vb, nxt, 0, cur, 3); \
        ITER(sB, sA, kfa0, kfa1, kfb0, kfb1, vb, va, nxt, 1, nxt, 0); } while (0)
#pragma nounroll
    for (int st = 0; st < 13; ++st) { int stv = st; asm volatile("" : "+s"(stv)); ATT_STAGE(true); }
#pragma nounroll
    for (int st = 13; st < 16; ++st) { int stv = st; asm volatile("" : "+s"(stv)); ATT_STAGE(false); }
    lsum += lsum2;
    unsigned long long ts2 = 0ull;
    int lz = threadIdx.x & 63; asm volatile("" : "+v"(lz));
    const int rr = lz & 31, hh = lz >> 5;
    float l;
    {
        const float l0 = __int_as_float(__builtin_amdgcn_ds_bpermute((rr & 15) << 2, __float_as_int(lsum[0]))), l1 = __int_as_float(__builtin_amdgcn_ds_bpermute((rr & 15) << 2, __float_as_int(lsum[1])));
        l = (rr & 16) ? l1 : l0;
    }
    const bool redo = __any(!(l >= 3.23117427e-27f));
    if (redo) {
        const char* Kg = (const char*)QKV + (size_t)(32 + head) * 131072 + rr * 64 + hh * 16;
        const u16* Vg = (const u16*)((const char*)QKV + (size_t)(64 + head) * 131072) + rr;
        const f32x16 zero = {};
        float m = -INFINITY;
#pragma nounroll
        for (int t = 0; t < 64; ++t) {
            const bf16x8 k0 = *(const bf16x8*)(Kg + (size_t)t * 2048), k1 = *(const bf16x8*)(Kg + (size_t)t * 2048 + 32);
            f32x16 sc2 = MFMA32(k0, qf0, zero); sc2 = MFMA32(k1, qf1, sc2);
#pragma unroll
            for (int r = 0; r < 16; ++r) m = fmaxf(m, sc2[r]);
        }
        m = max32(m);
        f32x16 nm;
#pragma unroll
        for (int r = 0; r < 16; ++r) nm[r] = -m;
        o = zero; float ls = 0.f;
#pragma nounroll
        for (int t = 0; t < 64; ++t) {
            const bf16x8 k0 = *(const bf16x8*)(Kg + (size_t)t * 2048), k1 = *(const bf16x8*)(Kg + (size_t)t * 2048 + 32);
            f32x16 sc2 = MFMA32(k0, qf0, nm); sc2 = MFMA32(k1, qf1, sc2);
#pragma unroll
            for (int r = 0; r < 16; ++r) sc2[r] = __builtin_amdgcn_exp2f(sc2[r]);
            const bf16x8 p0 = pack8(sc2, 0), p1 = pack8(sc2, 8);
            bf16x8 v0, v1;
#pragma unroll
            for (int j = 0; j < 8; ++j) {
                const int key = t * 32 + 8 * (j >> 2) + 4 * hh + (j & 3);
                v0[j] = (short)Vg[(size_t)key * 32]; v1[j] = (short)Vg[(size_t)(key + 16) * 32];
                ls += __uint_as_float((unsigned)(u16)p0[j] << 16) + __uint_as_float((unsigned)(u16)p1[j] << 16);
            }
            o = MFMA32(v0, p0, o); o = MFMA32(v1, p1, o);
        }
        l = add32(ls);
    }
    const float sc = gq / l;
    const int row = q0 + rr, fr = row & 15, mt = row >> 4;
    const __amdgpu_buffer_rsrc_t rs = __builtin_amdgcn_make_buffer_rsrc(O, 0, 2048 * 1024 * 2, 0x00020000);
    const unsigned ob = (unsigned)(mt * 32 + head) * 1024 + fr * 64;
    const int gsw = (-(fr >> 2)) & 3;
#pragma unroll
    for (int c = 0; c < 4; c += 2) {
        unsigned ax = cvtpk(o[4 * c] * sc, o[4 * c + 1] * sc), ay = cvtpk(o[4 * c + 2] * sc, o[4 * c + 3] * sc);
        unsigned bx = cvtpk(o[4 * c + 4] * sc, o[4 * c + 5] * sc), by = cvtpk(o[4 * c + 6] * sc, o[4 * c + 7] * sc);
        auto rx = __builtin_amdgcn_permlane32_swap(ax, bx, false, false); auto ry = __builtin_amdgcn_permlane32_swap(ay, by, false, false);
        const u32x4 w = {rx[0], ry[0], rx[1], ry[1]};
        __builtin_amdgcn_raw_buffer_store_b128(w, rs, ob + (((c + hh) ^ gsw) * 16), 0, 16);
    }
    asm volatile("s_waitcnt vmcnt(0) lgkmcnt(0)\n\ts_barrier" ::: "memory");
    if (threadIdx.x == 0) __hip_atomic_fetch_add(cnt + qb * 32, 1u, __ATOMIC_RELAXED, __HIP_MEMORY_SCOPE_AGENT);
}

#define G_RD(x, w, so, s) do { \
        x[0] = ldsb128(LP((aX0 + (so)) ^ ((s) * 32))); w[0] = vtr(LP(aW0 + (so)) + (s) * 2048); w[1] = vtr(LP(aW0 + (so)) + (s) * 2048 + 512); \
        x[1] = ldsb128(LP((aX0 + (so)) ^ ((s) * 32)) + 2048); w[2] = vtr(LP((aW0 + (so)) ^ 64) + (s) * 2048); w[3] = vtr(LP((aW0 + (so)) ^ 64) + (s) * 2048 + 512); } while (0)
#define G_MM(x, w) do { \
        acc[0][0] = MFMA32(cat8(w[0], w[1]), x[0], acc[0][0]); acc[1][0] = MFMA32(cat8(w[0], w[1]), x[1], acc[1][0]); \
        acc[0][1] = MFMA32(cat8(w[2], w[3]), x[0], acc[0][1]); acc[1][1] = MFMA32(cat8(w[2], w[3]), x[1], acc[1][1]); } while (0)
#define DBGADV pa += 1024; pb += 32 * 2048;
#define PRIO_ON
#define PRIO_OFF
#define ND ((112 - EXH) / 2)
#define PFD 6
__device__ __forceinline__ void gemm_role(const u16* __restrict__ H, const u16* __restrict__ wd, float* __restrict__ P, unsigned* __restrict__ flags, char* lds, int bid, unsigned long long* stamps)
{
    const int tid = threadIdx.x, lane = tid & 63;
    const int wid = __builtin_amdgcn_readfirstlane(tid >> 6);
    const int wm = wid & 1, wn = (wid >> 1) & 1, wk = wid >> 2;
    const int jb = bid - 256, xcd = jb & 7, idx = jb >> 3;
    const int ks = xcd & 1, pnl = idx >> 3, pml = idx & 7, pn = 4 * ((xcd >> 1) & 1) + pnl, pm = 8 * (xcd >> 2) + pml;
    const unsigned lds0 = (unsigned)(uintptr_t)lds;
    unsigned long long ts0 = 0ull, tr0 = 0ull, tA0, tA1;
    if (GEMM_PRIO) __builtin_amdgcn_s_setprio(GEMM_PRIO);
#define VW(n) asm volatile("s_waitcnt vmcnt(" #n ") lgkmcnt(0)\n\ts_barrier" ::: "memory")
    const int sr = lane >> 3, spos = lane & 7;
    const unsigned voffB = ((sr & 3) + 16 * (sr >> 2)) * 2048 + ((spos ^ (((sr >> 1) & 1) << 2)) * 16);
    const unsigned voffA = lane * 16;
    const unsigned ldsA = lds0 + wid * 1024, ldsB = lds0 + 8192 + wid * 1024;
    const char* pa = (const char*)H + ((size_t)(pm * 8 + wid) * 112 + ND * ks) * 1024;
    const char* pb = (const char*)wd + (size_t)(ND * ks * 32 + 4 * (wid & 3)) * 2048 + pn * 256 + (wid >> 2) * 128;
#define K4_ISSUE() do { const unsigned so_ = slot_i * 16384; glds16s(pa, voffA, ldsA + so_); glds16s(pb, voffB, ldsB + so_); pa += 1024; pb += 32 * 2048; slot_i = (slot_i == 4) ? 0 : slot_i + 1; } while (0)
    const int r32 = lane & 31, hi = lane >> 5, fr = lane & 15;
    const unsigned aX0 = lds0 + (wm * 4 + (r32 >> 4)) * 1024 + fr * 64 + ((hi ^ ((-(fr >> 2)) & 3)) * 16);
    const int tq = (lane & 15) >> 2, tp = lane & 3;
    const unsigned aW0 = lds0 + 8192 + wn * 4096 + (8 * hi + tq) * 128 + (((tq >> 1) & 1) << 6) + ((lane >> 4) & 1) * 32 + tp * 8;
    f32x16 acc[2][2];
#pragma unroll
    for (int m = 0; m < 2; ++m)
#pragma unroll
        for (int n = 0; n < 2; ++n) acc[m][n] = (f32x16){};
    bf16x8 xp[2] = {}, xq[2] = {}; s16x4 wp[4] = {}, wq[4] = {};
    int slot_i = 0;
#pragma unroll
    for (int i = 0; i < 5; ++i) K4_ISSUE();
    int rs_i = wk;
    VW(6);
    tA0 = 0ull;
    G_RD(xp, wp, rs_i * 16384, 0);
#define G_STEP(W, I1, I2) do { \
        const unsigned so = rs_i * 16384; \
        rs_i += 2; rs_i = (rs_i >= 5) ? rs_i - 5 : rs_i; \
        const unsigned sn = rs_i * 16384; \
        G_RD(xq, wq, so, 1); SBAR(); G_MM(xp, wp); SBAR(); \
        VW(W); \
        G_RD(xp, wp, sn, 0); SBAR(); if (I1) K4_ISSUE(); if (I2) K4_ISSUE(); SBAR(); PRIO_ON; G_MM(xq, wq); PRIO_OFF; SBAR(); } while (0)
#pragma nounroll
    for (int t = 0; t < ND / 2 - 3; ++t) { asm volatile("" : "+s"(rs_i), "+s"(slot_i)); G_STEP(2, true, true); }
    G_STEP(2, true, false);
    G_STEP(0, false, false);
    G_STEP(0, false, false);
    asm volatile("s_waitcnt lgkmcnt(0)\n\ts_barrier" ::: "memory");
    tA1 = 0ull;
    {
        int lz = threadIdx.x & 63; asm volatile("" : "+v"(lz));
        const int rr = lz & 31, hh = lz >> 5;
        f32x16 give0, give1, keep0, keep1;
        if (wk) { give0 = acc[0][0]; give1 = acc[0][1]; keep0 = acc[1][0]; keep1 = acc[1][1]; }
        else { give0 = acc[1][0]; give1 = acc[1][1]; keep0 = acc[0][0]; keep1 = acc[0][1]; }
        LAS f32x4* xs = (LAS f32x4*)(lds + ((wid & 3) * 2 + wk) * 8192) + lz;
#pragma unroll
        for (int g = 0; g < 4; ++g) {
            xs[g * 64] = (f32x4){give0[4 * g], give0[4 * g + 1], give0[4 * g + 2], give0[4 * g + 3]};
            xs[(4 + g) * 64] = (f32x4){give1[4 * g], give1[4 * g + 1], give1[4 * g + 2], give1[4 * g + 3]};
        }
        asm volatile("s_waitcnt lgkmcnt(0)\n\ts_barrier" ::: "memory");
        const LAS f32x4* xr = (const LAS f32x4*)(lds + ((wid & 3) * 2 + (1 - wk)) * 8192) + lz;
        const __amdgpu_buffer_rsrc_t rsP = __builtin_amdgcn_make_buffer_rsrc(P, 0, 8 << 20, 0x00020000);
        const unsigned pd = ((unsigned)(((ks * 16 + pm) * 8 + pn) * 8 + (wm * 2 + wk) * 2 + wn) * 256 + lz) * 16;
#pragma unroll
        for (int gp = 0; gp < 2; ++gp) {
            const int g = 2 * gp;
            const f32x4 a0 = (f32x4){keep0[4 * g], keep0[4 * g + 1], keep0[4 * g + 2], keep0[4 * g + 3]} + xr[g * 64];
            const f32x4 a1 = (f32x4){keep0[4 * g + 4], keep0[4 * g + 5], keep0[4 * g + 6], keep0[4 * g + 7]} + xr[(g + 1) * 64];
            const f32x4 b0 = (f32x4){keep1[4 * g], keep1[4 * g + 1], keep1[4 * g + 2], keep1[4 * g + 3]} + xr[(4 + g) * 64];
            const f32x4 b1 = (f32x4){keep1[4 * g + 4], keep1[4 * g + 5], keep1[4 * g + 6], keep1[4 * g + 7]} + xr[(4 + g + 1) * 64];
            const u32x4 wa = {cvtpk(a0[0], a0[1]), cvtpk(a0[2], a0[3]), cvtpk(a1[0], a1[1]), cvtpk(a1[2], a1[3])};
            const u32x4 wb = {cvtpk(b0[0], b0[1]), cvtpk(b0[2], b0[3]), cvtpk(b1[0], b1[1]), cvtpk(b1[2], b1[3])};
            __builtin_amdgcn_raw_buffer_store_b128(wa, rsP, pd + gp * 1024, 0, 16);
            __builtin_amdgcn_raw_buffer_store_b128(wb, rsP, pd + (2 + gp) * 1024, 0, 16);
        }
        asm volatile("s_waitcnt vmcnt(0)\n\ts_barrier" ::: "memory");
        if (threadIdx.x == 0) __hip_atomic_fetch_add(flags + (pm * 8 + pn) * 32, 1u, __ATOMIC_RELAXED, __HIP_MEMORY_SCOPE_AGENT);
        (void)rr; (void)hh;
    }
}

#define UNP(a, b, i) ((f32x4){__uint_as_float(a[i] << 16) + __uint_as_float(b[i] << 16), __uint_as_float(a[i] & 0xffff0000u) + __uint_as_float(b[i] & 0xffff0000u), \
                              __uint_as_float(a[(i) + 1] << 16) + __uint_as_float(b[(i) + 1] << 16), __uint_as_float(a[(i) + 1] & 0xffff0000u) + __uint_as_float(b[(i) + 1] & 0xffff0000u)})

__device__ __forceinline__ void wo_role(const u16* __restrict__ H, const u16* __restrict__ wd, const u16* __restrict__ Ob, const u16* __restrict__ wo, const float* __restrict__ P, const float* __restrict__ G, u16* __restrict__ out,
                                        unsigned* __restrict__ flags, unsigned* __restrict__ cnt, char* lds, int bid, unsigned long long* stamps)
{
    int tid = threadIdx.x; asm volatile("" : "+v"(tid));
    const int lane = tid & 63;
    const int wid = __builtin_amdgcn_readfirstlane(tid >> 6);
    const int wm = wid & 1, wn = (wid >> 1) & 1, wk = wid >> 2;
    const int xcd = bid & 7, idx = bid >> 3;
    const int rt = 8 * (xcd >> 1) + (idx & 7), pn = 4 * (xcd & 1) + (idx >> 3);
    const unsigned lds0 = (unsigned)(uintptr_t)lds;
    unsigned long long tw0 = 0ull, tw1, tw2, tw3;
    const int sr = lane >> 3, spos = lane & 7;
    const unsigned voffB = ((sr & 3) + 16 * (sr >> 2)) * 2048 + ((spos ^ (((sr >> 1) & 1) << 2)) * 16);
    const unsigned voffA = lane * 16;
    const char* pa = (const char*)H + ((size_t)(rt * 4 + (wid & 3)) * 112 + (112 - EXH) + (wid >> 2)) * 1024;
    const char* pb = (const char*)wd + (size_t)((112 - EXH) * 32 + 4 * (wid & 3)) * 2048 + pn * 256 + (wid >> 2) * 128;
    const char* paO = (const char*)Ob + ((size_t)(rt * 4 + (wid & 3)) * 32 + (wid >> 2)) * 1024;
    const char* pbO = (const char*)wo + (size_t)(4 * (wid & 3)) * 2048 + pn * 256 + (wid >> 2) * 128;
    int left = EXH / 2, sl = 0, sc = 0;
    const unsigned dA = lds0 + (wid >> 2) * 12288 + (wid & 3) * 1024, dB = lds0 + 4096 + wid * 1024;
#define K5_ISSUE() do { const unsigned so_ = sl * 24576; glds16s(pa, voffA, dA + so_); glds16s(pb, voffB, dB + so_); glds16s(pb + 32 * 2048, voffB, dB + so_ + 12288); \
                        pa += 2048; pb += (size_t)64 * 2048; sl = (sl == 2) ? 0 : sl + 1; --left; pa = (left == 0) ? paO : pa; pb = (left == 0) ? pbO : pb; } while (0)
    float gmv;
    { const unsigned go = (unsigned)(rt * 64 + wm * 32 + (lane & 31)) * 4; asm volatile("global_load_dword %0, %1, %2" : "=v"(gmv) : "v"(go), "s"(G) : "memory"); }
    unsigned touch = 0;
    K5_ISSUE(); K5_ISSUE(); K5_ISSUE();
    const int r32 = lane & 31, hi = lane >> 5, fr = lane & 15;
    const unsigned aX0 = lds0 + wk * 12288 + (wm * 2 + (r32 >> 4)) * 1024 + fr * 64 + ((hi ^ ((-(fr >> 2)) & 3)) * 16);
    const int tq = (lane & 15) >> 2, tp = lane & 3;
    const unsigned aW0 = lds0 + wk * 12288 + 4096 + wn * 4096 + (8 * hi + tq) * 128 + (((tq >> 1) & 1) << 6) + ((lane >> 4) & 1) * 32 + tp * 8;
    f32x16 acc[2] = {};
#define K5_COMP() do { const unsigned so = sc * 24576; \
        const bf16x8 x0 = ldsb128(LP(aX0 + so)), x1 = ldsb128(LP((aX0 + so) ^ 32)); \
        const s16x4 w0 = vtr(LP(aW0 + so)), w1 = vtr(LP(aW0 + so) + 512), w2 = vtr(LP((aW0 + so) ^ 64)), w3 = vtr(LP((aW0 + so) ^ 64) + 512); \
        const s16x4 w4 = vtr(LP(aW0 + so) + 2048), w5 = vtr(LP(aW0 + so) + 2560), w6 = vtr(LP((aW0 + so) ^ 64) + 2048), w7 = vtr(LP((aW0 + so) ^ 64) + 2560); \
        acc[0] = MFMA32(cat8(w0, w1), x0, acc[0]); acc[1] = MFMA32(cat8(w2, w3), x0, acc[1]); acc[0] = MFMA32(cat8(w4, w5), x1, acc[0]); acc[1] = MFMA32(cat8(w6, w7), x1, acc[1]); \
        sc = (sc == 2) ? 0 : sc + 1; } while (0)
#define K5_RUN(n_) do { _Pragma("nounroll") for (int s2 = 0; s2 < (n_); ++s2) { asm volatile("" : "+s"(sl), "+s"(sc), "+s"(left)); VW(3); K5_ISSUE(); K5_COMP(); } } while (0)
    VW(6); asm volatile("" : "+v"(gmv)); gmv = 1.0f - gmv; K5_COMP();
    K5_RUN(EXH / 2 - 3);
    if (wid == 0) {
        unsigned spins = 0;
        while (__builtin_amdgcn_readfirstlane(__hip_atomic_load(cnt + (rt >> 2) * 32, __ATOMIC_RELAXED, __HIP_MEMORY_SCOPE_AGENT)) < 32u) { __builtin_amdgcn_s_sleep(2); if (++spins > (1u << 22)) break; }
        __builtin_amdgcn_fence(__ATOMIC_ACQUIRE, "agent");
        asm volatile("s_waitcnt vmcnt(0)" ::: "memory");
    }
    tw1 = 0ull;
    asm volatile("s_waitcnt lgkmcnt(0)\n\ts_barrier" ::: "memory");
    K5_RUN(2);
    {
        acc[0] *= gmv; acc[1] *= gmv;
    }
    K5_RUN(14);
    VW(3); K5_COMP();
    VW(0); K5_COMP();
    asm volatile("" :: "v"(touch));
    tw2 = 0ull;
    if (wid == 0) {
        unsigned spins = 0;
        while (__builtin_amdgcn_readfirstlane(__hip_atomic_load(flags + ((rt >> 1) * 8 + pn) * 32, __ATOMIC_RELAXED, __HIP_MEMORY_SCOPE_AGENT)) < 2u) { __builtin_amdgcn_s_sleep(1); if (++spins > (1u << 22)) break; }
    }
    int lz = threadIdx.x & 63; asm volatile("" : "+v"(lz));
    const int rr = lz & 31, hh = lz >> 5;
    f32x16 give, keep; if (wk) { give = acc[0]; keep = acc[1]; } else { give = acc[1]; keep = acc[0]; }
    asm volatile("s_waitcnt vmcnt(0) lgkmcnt(0)\n\ts_barrier" ::: "memory");
    tw3 = 0ull;
    const __amdgpu_buffer_rsrc_t rsP = __builtin_amdgcn_make_buffer_rsrc((void*)P, 0, 8 << 20, 0x00020000);
    const unsigned blk = (unsigned)(((rt >> 1) * 8 + pn) * 8 + ((rt & 1) * 2 + wm) * 2 + wn);
    const unsigned p0 = (blk * 256 + wk * 128 + lz) * 16, p1 = p0 + 16 * 8 * 8 * 256 * 16;
    u32x4 pv[4];
    pv[0] = __builtin_amdgcn_raw_buffer_load_b128(rsP, p0, 0, 16); pv[1] = __builtin_amdgcn_raw_buffer_load_b128(rsP, p0 + 1024, 0, 16);
    pv[2] = __builtin_amdgcn_raw_buffer_load_b128(rsP, p1, 0, 16); pv[3] = __builtin_amdgcn_raw_buffer_load_b128(rsP, p1 + 1024, 0, 16);
    const float gm = gmv;
    LAS f32x4* xs = (LAS f32x4*)(lds + ((wid & 3) * 2 + wk) * 4096) + lz;
#pragma unroll
    for (int g = 0; g < 4; ++g) xs[g * 64] = (f32x4){give[4 * g], give[4 * g + 1], give[4 * g + 2], give[4 * g + 3]};
    asm volatile("s_waitcnt lgkmcnt(0)\n\ts_barrier" ::: "memory");
    const LAS f32x4* xr = (const LAS f32x4*)(lds + ((wid & 3) * 2 + (1 - wk)) * 4096) + lz;
    const int row = rt * 64 + wm * 32 + rr;
#pragma unroll
    for (int g = 0; g < 4; g += 2) {
        const f32x4 v0 = (f32x4){keep[4 * g], keep[4 * g + 1], keep[4 * g + 2], keep[4 * g + 3]} + xr[g * 64] + UNP(pv[g >> 1], pv[2 + (g >> 1)], 0) * gm;
        const f32x4 v1 = (f32x4){keep[4 * g + 4], keep[4 * g + 5], keep[4 * g + 6], keep[4 * g + 7]} + xr[(g + 1) * 64] + UNP(pv[g >> 1], pv[2 + (g >> 1)], 2) * gm;
        const unsigned ax = cvtpk(v0[0], v0[1]), ay = cvtpk(v0[2], v0[3]), bx = cvtpk(v1[0], v1[1]), by = cvtpk(v1[2], v1[3]);
        auto rx = __builtin_amdgcn_permlane32_swap(ax, bx, false, false); auto ry = __builtin_amdgcn_permlane32_swap(ay, by, false, false);
        const u32x4 w = {rx[0], ry[0], rx[1], ry[1]};
        const int col = pn * 128 + wn * 64 + wk * 32 + (g + hh) * 8;
        *(u32x4*)(out + (size_t)row * 1024 + col) = w;
    }
}

__global__ __launch_bounds__(512, 4) void k23_fused(const u16* __restrict__ QKV, const float* __restrict__ KMAX, const float* __restrict__ G, u16* __restrict__ O,
                                                    const u16* __restrict__ H, const u16* __restrict__ wd, const u16* __restrict__ wo, u16* __restrict__ out, float* __restrict__ P,
                                                    unsigned* __restrict__ flags, unsigned long long* stamps)
{
    extern __shared__ __attribute__((aligned(16))) char lds[];
    const int bid = blockIdx.x;
    unsigned* cnt = flags + 256 * 32;
    if (bid < 256) { attn_role(QKV, KMAX, G, O, cnt, lds, bid, stamps); wo_role(H, wd, O, wo, P, G, out, flags, cnt, lds, bid, stamps); }
    else gemm_role(H, wd, P, flags, lds, bid, stamps);
}

extern "C" void kernel_launch(void* const* d_in, const int* in_sizes, int n_in, void* d_out, int out_size, void* d_ws, size_t ws_size, hipStream_t stream)
{
    const u16* x = (const u16*)d_in[0]; const u16* wqkv = (const u16*)d_in[1]; const u16* wo = (const u16*)d_in[2];
    const u16* wg = (const u16*)d_in[4]; const u16* wu = (const u16*)d_in[5]; const u16* wd = (const u16*)d_in[6]; const u16* gw = (const u16*)d_in[7];
    char* ws = (char*)d_ws;
    u16* QKV = (u16*)ws;
    u16* H = (u16*)(ws + (16u << 20));
    u16* Ob = (u16*)(ws + (32u << 20));
    float* P = (float*)(ws + (40u << 20));
    float* G = (float*)(ws + (48u << 20));
    float* KM = (float*)(ws + (48u << 20) + 65536);
    unsigned* flags = (unsigned*)(ws + (48u << 20) + 131072);
    hipFuncSetAttribute((const void*)k1_gemm, hipFuncAttributeMaxDynamicSharedMemorySize, K1_LDS);
    hipFuncSetAttribute((const void*)k23_fused, hipFuncAttributeMaxDynamicSharedMemorySize, K23_LDS);
    hipLaunchKernelGGL(k1_gemm, dim3(256), dim3(512), K1_LDS, stream, x, wqkv, wg, wu, gw, QKV, H, G, KM, flags, wd, wo, (unsigned long long*)nullptr);
    hipLaunchKernelGGL(k23_fused, dim3(512), dim3(512), K23_LDS, stream, QKV, KM, G, Ob, H, wd, wo, (u16*)d_out, P, flags, (unsigned long long*)nullptr);
}
```

```cpp
#define K1_AUX 2
#define EXH 8
#define NO_TOUCH
#define ATT_PRIO 1
#define GELU_PK
#include <hip/hip_runtime.h>
#include <stdint.h>
typedef unsigned short u16;
typedef short bf16x8 __attribute__((ext_vector_type(8)));
typedef short s16x4 __attribute__((ext_vector_type(4)));
typedef float f32x4 __attribute__((ext_vector_type(4)));
typedef float f32x16 __attribute__((ext_vector_type(16)));
typedef unsigned u32x4 __attribute__((ext_vector_type(4)));
#define LAS __attribute__((address_space(3)))
typedef LAS const char* lds_cptr;
typedef short v4i16_t __attribute__((ext_vector_type(4)));
#define SBAR() __builtin_amdgcn_sched_barrier(0)

constexpr int S_ = 2048, D_ = 1024, F_ = 3584, NH_ = 32;
constexpr float QSCALE = 0.17677669529663687f * 1.4426950408889634f;

typedef __bf16 bf16x2_t __attribute__((ext_vector_type(2)));
typedef float f32x2_t __attribute__((ext_vector_type(2)));
__device__ __forceinline__ unsigned cvtpk(float lo, float hi) { const f32x2_t v = {lo, hi}; return __builtin_bit_cast(unsigned, __builtin_convertvector(v, bf16x2_t)); }
__device__ __forceinline__ void glds16s(const void* sbase, unsigned voff, unsigned lds_base) {
    asm volatile("s_mov_b32 m0, %2\n\ts_nop 0\n\tglobal_load_lds_dwordx4 %0, %1"
                 :: "v"(voff), "s"(sbase), "s"(lds_base) : "memory");
}
__device__ __forceinline__ s16x4 vtr(lds_cptr p) { return __builtin_bit_cast(s16x4, __builtin_amdgcn_ds_read_tr16_b64_v4i16((LAS v4i16_t*)p)); }
__device__ __forceinline__ bf16x8 cat8(s16x4 a, s16x4 b) { return (bf16x8){a[0], a[1], a[2], a[3], b[0], b[1], b[2], b[3]}; }
__device__ __forceinline__ bf16x8 ldsb128(lds_cptr p) { return *(const LAS bf16x8*)p; }

__device__ __forceinline__ lds_cptr LP(unsigned a) { return (lds_cptr)(uintptr_t)a; }
template <int X> __device__ __forceinline__ float swz_xor(float v) { return __int_as_float(__builtin_amdgcn_ds_swizzle(__float_as_int(v), (X << 10) | 0x1f)); }
__device__ __forceinline__ float add32(float v) { auto r = __builtin_amdgcn_permlane32_swap(__float_as_uint(v), __float_as_uint(v), false, false); return __uint_as_float(r[0]) + __uint_as_float(r[1]); }
__device__ __forceinline__ float max32(float v) { auto r = __builtin_amdgcn_permlane32_swap(__float_as_uint(v), __float_as_uint(v), false, false); return fmaxf(__uint_as_float(r[0]), __uint_as_float(r[1])); }
constexpr int K1_STAGE = 73728, K1_LDS = 2 * K1_STAGE;

__device__ __forceinline__ float gelu_mul(float g, float u) {
    const float t = g * g;
    const float p = __builtin_fmaf(t, -0.1029432397f, -2.302208198f);
    const float e = __builtin_amdgcn_exp2f(g * p);
    const float r = __builtin_amdgcn_rcpf(1.0f + e);
    return g * u * r;
}

__device__ __forceinline__ unsigned gelu_mul2(float g0, float g1, float u0, float u1) {
    const f32x2_t g = {g0, g1}, u = {u0, u1};
    const f32x2_t t = g * g;
    const f32x2_t p = t * -0.1029432397f + -2.302208198f;
    const f32x2_t z = g * p;
    f32x2_t e; e[0] = __builtin_amdgcn_exp2f(z[0]); e[1] = __builtin_amdgcn_exp2f(z[1]);
    const f32x2_t d = e + 1.0f;
    f32x2_t r; r[0] = __builtin_amdgcn_rcpf(d[0]); r[1] = __builtin_amdgcn_rcpf(d[1]);
    const f32x2_t h = (g * u) * r;
    return __builtin_bit_cast(unsigned, __builtin_convertvector(h, bf16x2_t));
}

__global__ __launch_bounds__(512, 2) void k1_gemm(const u16* __restrict__ x, const u16* __restrict__ wqkv, const u16* __restrict__ wg, const u16* __restrict__ wu,
                                                  const u16* __restrict__ gw, u16* __restrict__ QKV, u16* __restrict__ H, float* __restrict__ G, float* __restrict__ KMAX, unsigned* __restrict__ flags, const u16* __restrict__ wd, const u16* __restrict__ wo, unsigned long long* stamps)
{
    extern __shared__ __attribute__((aligned(16))) char lds[];
    const int tid = threadIdx.x, lane = tid & 63;
    const int wid = __builtin_amdgcn_readfirstlane(tid >> 6);
    const int wm = wid & 3, wn = wid >> 2;
    const int fr = lane & 15, fq = lane >> 4;
    const int bid = blockIdx.x, xcd = bid & 7, idx = bid >> 3, pm = idx & 7, pn = xcd * 4 + (idx >> 3);
    const bool typeB = pn >= 24;
    const unsigned lds0 = (unsigned)(uintptr_t)lds;
    unsigned long long ts0=0ull, tr0=0ull;

    if (bid == 0) { for (int i = tid; i < 264 * 32; i += 512) flags[i] = 0u; }
    const char* cb0; const char* cb1; const char* cb2; const char* cb3; const char* cb4;
    unsigned ldmid;
    if (!typeB) { cb0 = (const char*)(wg + 128 * pn); cb1 = (const char*)(wu + 128 * pn); cb2 = cb0 + 128; cb3 = cb1 + 128; cb4 = (const char*)(wqkv + 64 * pn); ldmid = F_ * 2; }
    else { const int i = pn - 24; cb0 = (const char*)(wg + 3072 + 64 * i); cb1 = (const char*)(wu + 3072 + 64 * i); cb2 = (const char*)(wqkv + 64 * (24 + 3 * i)); cb3 = cb2 + 128; cb4 = cb2 + 256; ldmid = 3072 * 2; }
    cb0 += (size_t)(8 * wid) * (F_ * 2); cb1 += (size_t)(8 * wid) * (F_ * 2); cb2 += (size_t)(8 * wid) * ldmid; cb3 += (size_t)(8 * wid) * ldmid; cb4 += (size_t)(8 * wid) * (3072 * 2);
    const char* ca = (const char*)x + (size_t)(256 * pm + 8 * wid) * 2048;
    const int sr = lane >> 3, spos = lane & 7;
    const unsigned fB = ((sr >> 1) & 1) | ((wid & 1) << 1);
    const unsigned colB = (((spos >> 1) ^ fB) * 32) + (spos & 1) * 16;
    const unsigned voffG = sr * (F_ * 2) + colB, voffM = sr * ldmid + colB, voffQ = sr * (3072 * 2) + colB;
    const unsigned voffA = sr * 2048 + ((spos ^ (4 * (wid & 1) + (sr >> 1))) * 16);
    const unsigned ldsw = lds0 + wid * 1024;
    float gsig;

#define K1_PIECE(i, sb) do { \
        if ((i) < 4) glds16s(ca + (size_t)(i) * 131072, voffA, (sb) + (i) * 8192); \
        else if ((i) == 4) glds16s(cb0, voffG, (sb) + 32768); \
        else if ((i) == 5) glds16s(cb2, voffM, (sb) + 32768 + 16384); \
        else if ((i) == 6) glds16s(cb1, voffG, (sb) + 32768 + 8192); \
        else if ((i) == 7) glds16s(cb3, voffM, (sb) + 32768 + 24576); \
        else glds16s(cb4, voffQ, (sb) + 32768 + 32768); } while (0)
#define K1_ADV1() do { ca += 128; cb0 += 64 * F_ * 2; cb2 += (size_t)64 * ldmid; } while (0)
#define K1_ADV2() do { cb1 += 64 * F_ * 2; cb3 += (size_t)64 * ldmid; cb4 += 64 * 3072 * 2; } while (0)

    const int grow = 256 * pm + 8 * pn + wid;
    u32x4 gx0, gx1, gg0, gg1;
    {
        const char* xr = (const char*)(x + (size_t)grow * D_);
        const unsigned go = lane * 32;
        asm volatile("global_load_dwordx4 %0, %4, %5\n\tglobal_load_dwordx4 %1, %4, %5 offset:16\n\tglobal_load_dwordx4 %2, %4, %6\n\tglobal_load_dwordx4 %3, %4, %6 offset:16"
                     : "=&v"(gx0), "=&v"(gx1), "=&v"(gg0), "=&v"(gg1) : "v"(go), "s"(xr), "s"(gw) : "memory");
    }
#pragma unroll
    for (int i = 0; i < 9; ++i) K1_PIECE(i, ldsw);
    K1_ADV1(); K1_ADV2();
#pragma unroll
    for (int i = 0; i < 6; ++i) K1_PIECE(i, ldsw + K1_STAGE);
    K1_ADV1();
#define GWAIT 15
    unsigned long long tsa=0ull;
#define STR2(x) #x
#define STR(x) STR2(x)
    asm volatile("s_waitcnt vmcnt(" STR(GWAIT) ")" : "+v"(gx0), "+v"(gx1), "+v"(gg0), "+v"(gg1) :: "memory");
    {
        float s = 0.f;
#pragma unroll
        for (int j = 0; j < 4; ++j) {
            s += __uint_as_float(gx0[j] << 16) * __uint_as_float(gg0[j] << 16) + __uint_as_float(gx0[j] & 0xffff0000u) * __uint_as_float(gg0[j] & 0xffff0000u);
            s += __uint_as_float(gx1[j] << 16) * __uint_as_float(gg1[j] << 16) + __uint_as_float(gx1[j] & 0xffff0000u) * __uint_as_float(gg1[j] & 0xffff0000u);
        }
        s += swz_xor<1>(s); s += swz_xor<2>(s); s += swz_xor<4>(s); s += swz_xor<8>(s); s += swz_xor<16>(s); s = add32(s);
        gsig = 1.0f / (1.0f + __expf(-s));
    }

    unsigned long long tsb=0ull;
    unsigned aA0 = lds0 + (64 * wm + fr) * 128 + (((0 + fq) ^ (fr >> 1)) * 16);
    unsigned aA1 = lds0 + (64 * wm + fr) * 128 + (((4 + fq) ^ (fr >> 1)) * 16);
    const int tq = (lane & 15) >> 2, tp = lane & 3;
    const unsigned fR = ((tq >> 1) & 1) | ((fq & 1) << 1);
    const unsigned bbase = lds0 + 32768 + (8 * fq + tq) * 128 + 8 * tp;
    unsigned aB0 = bbase + 2 * wn * 8192 + ((0 ^ fR) << 5);
    unsigned aB1 = bbase + 2 * wn * 8192 + ((1 ^ fR) << 5);
    unsigned aB2 = bbase + 2 * wn * 8192 + ((2 ^ fR) << 5);
    unsigned aB3 = bbase + 2 * wn * 8192 + ((3 ^ fR) << 5);
    unsigned aB4a = bbase + 4 * 8192 + (((2 * wn) ^ fR) << 5);
    unsigned aB4b = bbase + 4 * 8192 + (((2 * wn + 1) ^ fR) << 5);

    f32x4 acc[4][10];
#pragma unroll
    for (int m = 0; m < 4; ++m)
#pragma unroll
        for (int s = 0; s < 10; ++s) acc[m][s] = (f32x4){0.f, 0.f, 0.f, 0.f};
    bf16x8 fa0[4], fa1[4];
    s16x4 bx[4], by[4];

#define MF(fb, h, fa, m, ti) acc[m][(ti) + (h)] = __builtin_amdgcn_mfma_f32_16x16x32_bf16(cat8(fb[2 * (h)], fb[2 * (h) + 1]), fa[m], acc[m][(ti) + (h)], 0, 0, 0)
#define RB(dst, j, aa, ab, off) dst[j] = vtr(LP(((j) < 2) ? (aa) : (ab)) + (off) + ((j) & 1) * 512)
#define RA(dst, m, a) dst[m] = ldsb128(LP(a) + (m) * 2048)
#define SUBP(cur, fa, ti, nxt, aa, ab, off, AR, GL) do { \
        MF(cur, 0, fa, 0, ti); SBAR(); RB(nxt, 0, aa, ab, off); SBAR(); \
        MF(cur, 1, fa, 0, ti); SBAR(); RB(nxt, 1, aa, ab, off); SBAR(); \
        MF(cur, 0, fa, 1, ti); SBAR(); RB(nxt, 2, aa, ab, off); SBAR(); \
        MF(cur, 1, fa, 1, ti); SBAR(); RB(nxt, 3, aa, ab, off); SBAR(); \
        MF(cur, 0, fa, 2, ti); SBAR(); AR(0); GL; SBAR(); \
        MF(cur, 1, fa, 2, ti); SBAR(); AR(1); SBAR(); \
        MF(cur, 0, fa, 3, ti); SBAR(); AR(2); SBAR(); \
        MF(cur, 1, fa, 3, ti); SBAR(); AR(3); SBAR(); } while (0)
#define NOA(m) do { } while (0)
#define RA0(m) RA(fa0, m, aA0)
#define RA1(m) RA(fa1, m, aA1)
#define NOG do { } while (0)
#define WBAR(n) asm volatile("s_waitcnt vmcnt(" #n ") lgkmcnt(0)\n\ts_barrier" ::: "memory")

    unsigned warm0 = 0;
    const int wgi = bid * 8 + wid;
    const char* wp = wgi < 1275 ? (const char*)wd + min(wgi * 5760, 7340032 - 5760) : (const char*)wo + min((wgi - 1275) * 5760, 2097152 - 5760);
    const unsigned wvo = (lane % 6) * 64;
    asm volatile("global_load_dword %0, %1, %2" : "+v"(warm0) : "v"(wvo), "s"(wp) : "memory"); wp += 384;
    K1_PIECE(6, ldsw + K1_STAGE);
    asm volatile("s_waitcnt vmcnt(11)\n\ts_barrier" ::: "memory");
    if (lane == 0) G[grow] = gsig;
#pragma unroll
    for (int j = 0; j < 4; ++j) RB(bx, j, aB0, aB1, 0);
#pragma unroll
    for (int m = 0; m < 4; ++m) RA0(m);
    int delta = K1_STAGE;
    unsigned long long ts1=0ull; tsa=0ull;

#define WBARM() WBAR(10)
#define VADD(a) asm volatile("v_add_u32 %0, %1, %0" : "+v"(a) : "s"(delta))
#define STEP(MAIN) do { \
        SUBP(bx, fa0, 0, by, aB0, aB1, 4096, RA1, if (MAIN || p2) K1_PIECE(7, ns)); \
        SUBP(by, fa1, 0, bx, aB2, aB3, 0, NOA, if (MAIN || p2) { K1_PIECE(8, ns); K1_ADV2(); }); \
        SUBP(bx, fa0, 2, by, aB2, aB3, 4096, NOA, NOG); \
        if (MAIN) WBARM(); else if (p2) WBAR(9); else WBAR(0); \
        SUBP(by, fa1, 2, bx, aB0, aB1, 8192, NOA, if (MAIN) K1_PIECE(0, cs)); \
        SUBP(bx, fa0, 4, by, aB0, aB1, 8192 + 4096, NOA, if (MAIN) K1_PIECE(1, cs)); \
        SUBP(by, fa1, 4, bx, aB2, aB3, 8192, NOA, if (MAIN) K1_PIECE(2, cs)); \
        SUBP(bx, fa0, 6, by, aB2, aB3, 8192 + 4096, NOA, if (MAIN) K1_PIECE(3, cs)); \
        SUBP(by, fa1, 6, bx, aB4a, aB4b, 0, NOA, if (MAIN) K1_PIECE(4, cs)); \
        SUBP(bx, fa0, 8, by, aB4a, aB4b, 4096, NOA, if (MAIN) { K1_PIECE(5, cs); K1_ADV1(); }); \
        VADD(aA0); VADD(aA1); VADD(aB0); VADD(aB1); VADD(aB2); VADD(aB3); \
        if (MAIN) WBARM(); else WBAR(3); \
        SUBP(by, fa1, 8, bx, aB0, aB1, 0, RA0, if (MAIN) K1_PIECE(6, cs)); \
        VADD(aB4a); VADD(aB4b); delta = -delta; { const unsigned tmp = cs; cs = ns; ns = tmp; } \
    } while (0)
    unsigned cs = ldsw, ns = ldsw + K1_STAGE;
#pragma nounroll
    for (int tt = 0; tt < 14; ++tt) {
        const bool p2 = true; STEP(true); asm volatile("" : "+s"(cs), "+s"(ns), "+s"(delta));
        asm volatile("global_load_dword %0, %1, %2" : "+v"(warm0) : "v"(wvo), "s"(wp) : "memory"); wp += 384;
    }
#pragma nounroll
    for (int tt = 0; tt < 2; ++tt) { int t = tt; asm volatile("" : "+s"(t)); const bool p2 = t == 0; STEP(false); asm volatile("" : "+s"(cs), "+s"(ns), "+s"(delta)); }
    unsigned long long ts2=0ull; tsb=0ull;

    const int row0 = 256 * pm + 64 * wm;
    const __amdgpu_buffer_rsrc_t rsH = __builtin_amdgcn_make_buffer_rsrc(H, 0, 2048 * 3584 * 2, 0x00020000), rsQ = __builtin_amdgcn_make_buffer_rsrc(QKV, 0, 96 * 2048 * 64, 0x00020000);
#define ST16(base, rs, off, w) __builtin_amdgcn_raw_buffer_store_b128(w, rs, off, 0, K1_AUX)
    const unsigned swz = ((fq ^ ((-(fr >> 2)) & 3)) * 16) + fr * 64;
    const bool gu = !typeB || wn == 0;
    if (gu) {
        const int hc0 = typeB ? (3072 + 64 * (pn - 24)) : (128 * pn + 64 * wn);
#pragma unroll
        for (int m = 0; m < 4; ++m) {
            const int mt = (row0 >> 4) + m;
#pragma unroll
            for (int i = 0; i < 2; ++i) {
                u32x4 w;
                { const f32x4 g = acc[m][2 * i], u = acc[m][4 + 2 * i];
                  w[0] = gelu_mul2(g[0], g[1], u[0], u[1]); w[1] = gelu_mul2(g[2], g[3], u[2], u[3]); }
                { const f32x4 g = acc[m][2 * i + 1], u = acc[m][4 + 2 * i + 1];
                  w[2] = gelu_mul2(g[0], g[1], u[0], u[1]); w[3] = gelu_mul2(g[2], g[3], u[2], u[3]); }
                ST16(H, rsH, ((unsigned)mt * 112 + (hc0 >> 5) + i) * 1024 + swz, w);
            }
        }
    }
    auto store_head = [&](int hb, const f32x4 (&ta)[4], const f32x4 (&tb)[4]) {
        const int which = hb >> 5;
        const float sc = which == 0 ? QSCALE : 1.0f;
        float kn = 0.f;
#pragma unroll
        for (int m = 0; m < 4; ++m) {
            const f32x4 a = ta[m] * sc, b = tb[m] * sc;
            u32x4 w; w[0] = cvtpk(a[0], a[1]); w[1] = cvtpk(a[2], a[3]); w[2] = cvtpk(b[0], b[1]); w[3] = cvtpk(b[2], b[3]);
            ST16(QKV, rsQ, ((unsigned)hb * 2048 + row0 + 16 * m + fr) * 64 + fq * 16, w);
            if (which == 1) {
                float n2 = a[0] * a[0] + a[1] * a[1] + a[2] * a[2] + a[3] * a[3] + b[0] * b[0] + b[1] * b[1] + b[2] * b[2] + b[3] * b[3];
                n2 += swz_xor<16>(n2); n2 = add32(n2);
                kn = fmaxf(kn, n2);
            }
        }
        if (which == 1) {
            kn = fmaxf(kn, swz_xor<8>(kn)); kn = fmaxf(kn, swz_xor<4>(kn)); kn = fmaxf(kn, swz_xor<2>(kn)); kn = fmaxf(kn, swz_xor<1>(kn));
            if (lane == 0) KMAX[(hb & 31) * 32 + pm * 4 + wm] = kn;
        }
    };
    {
        const int qc4 = typeB ? (24 + 3 * (pn - 24) + 2) : pn;
        const f32x4 ta[4] = {acc[0][8], acc[1][8], acc[2][8], acc[3][8]};
        const f32x4 tb[4] = {acc[0][9], acc[1][9], acc[2][9], acc[3][9]};
        store_head(2 * qc4 + wn, ta, tb);
    }
    if (!gu) {
        const int qc2 = 24 + 3 * (pn - 24);
#pragma unroll
        for (int c = 0; c < 2; ++c)
#pragma unroll
            for (int hh = 0; hh < 2; ++hh) {
                const f32x4 ta[4] = {acc[0][4 * c + 2 * hh], acc[1][4 * c + 2 * hh], acc[2][4 * c + 2 * hh], acc[3][4 * c + 2 * hh]};
                const f32x4 tb[4] = {acc[0][4 * c + 2 * hh + 1], acc[1][4 * c + 2 * hh + 1], acc[2][4 * c + 2 * hh + 1], acc[3][4 * c + 2 * hh + 1]};
                store_head(2 * (qc2 + c) + hh, ta, tb);
            }
    }
    asm volatile("" :: "v"(warm0));
}

#define MFMA32(a, b, c) __builtin_amdgcn_mfma_f32_32x32x16_bf16(a, b, c, 0, 0, 0)
#define MFMA16(a, b, c) __builtin_amdgcn_mfma_f32_16x16x32_bf16(a, b, c, 0, 0, 0)
constexpr int K2_SLOT = 16384, K2_NS = 4, K2_LDS = K2_SLOT * K2_NS;

__device__ __forceinline__ bf16x8 pack8(const f32x16& p, int b) {
    u32x4 w; w[0] = cvtpk(p[b], p[b + 1]); w[1] = cvtpk(p[b + 2], p[b + 3]); w[2] = cvtpk(p[b + 4], p[b + 5]); w[3] = cvtpk(p[b + 6], p[b + 7]);
    return __builtin_bit_cast(bf16x8, w);
}

#define K2_ISSUE(st) do { glds16s(Kh + (size_t)(st) * 8192, voffK, ldsw + ((st) & 3) * K2_SLOT); glds16s(Vh + (size_t)(st) * 8192, voffV, ldsw + ((st) & 3) * K2_SLOT + 8192); } while (0)
#define RDK(d0, d1, slot, i) do { d0 = ldsb128(LP(aK0 + (slot)) + (i) * 2048); d1 = ldsb128(LP(aK1 + (slot)) + (i) * 2048); } while (0)
#define QK(S, k0, k1) do { S = MFMA32(k0, qf0, negm); S = MFMA32(k1, qf1, S); } while (0)
#define PIN(x) asm volatile("" : "+v"(x))
#define PKA(a, S, i) a += (f32x2_t){S[i], S[(i) + 1]}
#define SUM0 do { PKA(ls0, SCUR, 0); PKA(ls1, SCUR, 2); PKA(ls0, SCUR, 4); PKA(ls1, SCUR, 6); PIN(ls0); PIN(ls1); } while (0)
#define SUM1 do { PKA(ls0, SCUR, 8); PKA(ls1, SCUR, 10); PKA(ls0, SCUR, 12); PKA(ls1, SCUR, 14); PIN(ls0); PIN(ls1); } while (0)
#define SUM0 lsum = MFMA16(sel, p0, lsum)
#define SUM1 lsum2 = MFMA16(sel, p1, lsum2)
#define EX4(S, b) do { S[b] = __builtin_amdgcn_exp2f(S[b]); S[b + 1] = __builtin_amdgcn_exp2f(S[b + 1]); S[b + 2] = __builtin_amdgcn_exp2f(S[b + 2]); S[b + 3] = __builtin_amdgcn_exp2f(S[b + 3]); } while (0)
#define RDV(d, slot, i) do { d[0] = vtr(LP(aV + (slot)) + (i) * 2048); d[1] = vtr(LP(aV + (slot)) + (i) * 2048 + 512); d[2] = vtr(LP(aV + (slot)) + (i) * 2048 + 1024); d[3] = vtr(LP(aV + (slot)) + (i) * 2048 + 1536); } while (0)
#define ITER(SC, SN, k0, k1, n0, n1, vc, vn, kslot, ki, vslot, vi) do { \
        f32x16& SCUR = SC; (void)SCUR; \
        SN = MFMA32(k0, qf0, negm); SBAR(); \
        EX4(SC, 0); PIN(SC); SBAR(); \
        SN = MFMA32(k1, qf1, SN); SBAR(); \
        RDK(n0, n1, kslot, ki); SBAR(); \
        EX4(SC, 4); const bf16x8 p0 = pack8(SC, 0); SBAR(); \
        o = MFMA32(cat8(vc[0], vc[1]), p0, o); SBAR(); \
        EX4(SC, 8); PIN(SC); SBAR(); \
        SUM0; SBAR(); \
        EX4(SC, 12); const bf16x8 p1 = pack8(SC, 8); SBAR(); \
        o = MFMA32(cat8(vc[2], vc[3]), p1, o); SBAR(); \
        RDV(vn, vslot, vi); SBAR(); \
        SUM1; SBAR(); \
    } while (0)
#define WB2(n) asm volatile("s_waitcnt vmcnt(" #n ") lgkmcnt(0)\n\ts_barrier" ::: "memory")
constexpr int K23_LDS = 81920;
#define GEMM_PRIO 0

__device__ __forceinline__ void attn_role(const u16* __restrict__ QKV, const float* __restrict__ KMAX, const float* __restrict__ G, u16* __restrict__ O, unsigned* __restrict__ cnt, char* lds, int bid, unsigned long long* stamps)
{
    const int tid = threadIdx.x, lane = tid & 63;
    const int wid = __builtin_amdgcn_readfirstlane(tid >> 6);
    const int r32 = lane & 31, hi = lane >> 5;
    const int xcd = bid & 7, idx = bid >> 3, head = 4 * xcd + (idx >> 3), qb = idx & 7;
    const unsigned lds0 = (unsigned)(uintptr_t)lds;
    unsigned long long ts0 = 0ull, tr0 = 0ull;
    if (ATT_PRIO) __builtin_amdgcn_s_setprio(ATT_PRIO);
    const char* Qh = (const char*)QKV + (size_t)head * 131072;
    const char* Kh = (const char*)QKV + (size_t)(32 + head) * 131072 + wid * 1024;
    const char* Vh = (const char*)QKV + (size_t)(64 + head) * 131072 + wid * 1024;
    const unsigned voffK = (lane >> 2) * 64 + (((lane & 3) ^ ((lane >> 4) & 3)) * 16), voffV = lane * 16;
    const unsigned ldsw = lds0 + wid * 1024;
    K2_ISSUE(0); K2_ISSUE(1);
    const int q0 = qb * 256 + wid * 32;
    const bf16x8 qf0 = *(const bf16x8*)(Qh + (size_t)(q0 + r32) * 64 + hi * 16);
    const bf16x8 qf1 = *(const bf16x8*)(Qh + (size_t)(q0 + r32) * 64 + 32 + hi * 16);
    const float kmx = KMAX[head * 32 + r32];
    float gq = G[q0 + r32];
    float qn2 = 0.f;
#pragma unroll
    for (int j = 0; j < 8; ++j) { const float a = __uint_as_float((unsigned)(u16)qf0[j] << 16), b = __uint_as_float((unsigned)(u16)qf1[j] << 16); qn2 += a * a + b * b; }
    qn2 = add32(qn2);
    float km2 = kmx; km2 = fmaxf(km2, swz_xor<1>(km2)); km2 = fmaxf(km2, swz_xor<2>(km2)); km2 = fmaxf(km2, swz_xor<4>(km2)); km2 = fmaxf(km2, swz_xor<8>(km2)); km2 = fmaxf(km2, swz_xor<16>(km2));
    const float mb = sqrtf(qn2 * km2) * 1.01f;
    f32x16 negm;
#pragma unroll
    for (int r = 0; r < 16; ++r) negm[r] = -mb;
    asm volatile("" : "+v"(negm), "+v"(gq));
    bf16x8 sel;
    { const short one = ((lane & 15) == ((lane >> 4) & 1)) ? (short)0x3F80 : (short)0;
#pragma unroll
      for (int j = 0; j < 8; ++j) sel[j] = one; }
    const unsigned aK0 = lds0 + r32 * 64 + (((0 + hi) ^ ((r32 >> 2) & 3)) * 16);
    const unsigned aK1 = lds0 + r32 * 64 + (((2 + hi) ^ ((r32 >> 2) & 3)) * 16);
    const unsigned aV = lds0 + 8192 + (4 * hi + ((lane & 15) >> 2)) * 64 + ((lane >> 4) & 1) * 32 + (lane & 3) * 8;
    f32x16 o = {}; f32x4 lsum = {0.f, 0.f, 0.f, 0.f}, lsum2 = {0.f, 0.f, 0.f, 0.f}; f32x2_t ls0 = {0.f, 0.f}, ls1 = {0.f, 0.f};
    f32x16 sA, sB;
    bf16x8 kfa0, kfa1, kfb0, kfb1;
    s16x4 va[4], vb[4];
    WB2(2);
    K2_ISSUE(2);
    RDK(kfa0, kfa1, 0, 0); RDK(kfb0, kfb1, 0, 1); RDV(va, 0, 0);
    QK(sA, kfa0, kfa1);
    unsigned long long ts1 = 0ull;
#define ATT_STAGE(MAIN) do { \
        const unsigned cur = (stv & 3) * K2_SLOT, nxt = ((stv + 1) & 3) * K2_SLOT; \
        ITER(sA, sB, kfb0, kfb1, kfa0, kfa1, va, vb, cur, 2, cur, 1); \
        ITER(sB, sA, kfa0, kfa1, kfb0, kfb1, vb, va, cur, 3, cur, 2); \
        if (MAIN) { WB2(2); K2_ISSUE(stv + 3); } else WB2(0); \
        ITER(sA, sB, kfb0, kfb1, kfa0, kfa1, va, vb, nxt, 0, cur, 3); \
        ITER(sB, sA, kfa0, kfa1, kfb0, kfb1, vb, va, nxt, 1, nxt, 0); } while (0)
#pragma nounroll
    for (int st = 0; st < 13; ++st) { int stv = st; asm volatile("" : "+s"(stv)); ATT_STAGE(true); }
#pragma nounroll
    for (int st = 13; st < 16; ++st) { int stv = st; asm volatile("" : "+s"(stv)); ATT_STAGE(false); }
    lsum += lsum2;
    unsigned long long ts2 = 0ull;
    int lz = threadIdx.x & 63; asm volatile("" : "+v"(lz));
    const int rr = lz & 31, hh = lz >> 5;
    float l;
    {
        const float l0 = __int_as_float(__builtin_amdgcn_ds_bpermute((rr & 15) << 2, __float_as_int(lsum[0]))), l1 = __int_as_float(__builtin_amdgcn_ds_bpermute((rr & 15) << 2, __float_as_int(lsum[1])));
        l = (rr & 16) ? l1 : l0;
    }
    const bool redo = __any(!(l >= 3.23117427e-27f));
    if (redo) {
        const char* Kg = (const char*)QKV + (size_t)(32 + head) * 131072 + rr * 64 + hh * 16;
        const u16* Vg = (const u16*)((const char*)QKV + (size_t)(64 + head) * 131072) + rr;
        const f32x16 zero = {};
        float m = -INFINITY;
#pragma nounroll
        for (int t = 0; t < 64; ++t) {
            const bf16x8 k0 = *(const bf16x8*)(Kg + (size_t)t * 2048), k1 = *(const bf16x8*)(Kg + (size_t)t * 2048 + 32);
            f32x16 sc2 = MFMA32(k0, qf0, zero); sc2 = MFMA32(k1, qf1, sc2);
#pragma unroll
            for (int r = 0; r < 16; ++r) m = fmaxf(m, sc2[r]);
        }
        m = max32(m);
        f32x16 nm;
#pragma unroll
        for (int r = 0; r < 16; ++r) nm[r] = -m;
        o = zero; float ls = 0.f;
#pragma nounroll
        for (int t = 0; t < 64; ++t) {
            const bf16x8 k0 = *(const bf16x8*)(Kg + (size_t)t * 2048), k1 = *(const bf16x8*)(Kg + (size_t)t * 2048 + 32);
            f32x16 sc2 = MFMA32(k0, qf0, nm); sc2 = MFMA32(k1, qf1, sc2);
#pragma unroll
            for (int r = 0; r < 16; ++r) sc2[r] = __builtin_amdgcn_exp2f(sc2[r]);
            const bf16x8 p0 = pack8(sc2, 0), p1 = pack8(sc2, 8);
            bf16x8 v0, v1;
#pragma unroll
            for (int j = 0; j < 8; ++j) {
                const int key = t * 32 + 8 * (j >> 2) + 4 * hh + (j & 3);
                v0[j] = (short)Vg[(size_t)key * 32]; v1[j] = (short)Vg[(size_t)(key + 16) * 32];
                ls += __uint_as_float((unsigned)(u16)p0[j] << 16) + __uint_as_float((unsigned)(u16)p1[j] << 16);
            }
            o = MFMA32(v0, p0, o); o = MFMA32(v1, p1, o);
        }
        l = add32(ls);
    }
    const float sc = gq / l;
    const int row = q0 + rr, fr = row & 15, mt = row >> 4;
    const __amdgpu_buffer_rsrc_t rs = __builtin_amdgcn_make_buffer_rsrc(O, 0, 2048 * 1024 * 2, 0x00020000);
    const unsigned ob = (unsigned)(mt * 32 + head) * 1024 + fr * 64;
    const int gsw = (-(fr >> 2)) & 3;
#pragma unroll
    for (int c = 0; c < 4; c += 2) {
        unsigned ax = cvtpk(o[4 * c] * sc, o[4 * c + 1] * sc), ay = cvtpk(o[4 * c + 2] * sc, o[4 * c + 3] * sc);
        unsigned bx = cvtpk(o[4 * c + 4] * sc, o[4 * c + 5] * sc), by = cvtpk(o[4 * c + 6] * sc, o[4 * c + 7] * sc);
        auto rx = __builtin_amdgcn_permlane32_swap(ax, bx, false, false); auto ry = __builtin_amdgcn_permlane32_swap(ay, by, false, false);
        const u32x4 w = {rx[0], ry[0], rx[1], ry[1]};
        __builtin_amdgcn_raw_buffer_store_b128(w, rs, ob + (((c + hh) ^ gsw) * 16), 0, 16);
    }
    asm volatile("s_waitcnt vmcnt(0) lgkmcnt(0)\n\ts_barrier" ::: "memory");
    if (threadIdx.x == 0) __hip_atomic_fetch_add(cnt + qb * 32, 1u, __ATOMIC_RELAXED, __HIP_MEMORY_SCOPE_AGENT);
}

#define G_RD(x, w, so, s) do { \
        x[0] = ldsb128(LP((aX0 + (so)) ^ ((s) * 32))); w[0] = vtr(LP(aW0 + (so)) + (s) * 2048); w[1] = vtr(LP(aW0 + (so)) + (s) * 2048 + 512); \
        x[1] = ldsb128(LP((aX0 + (so)) ^ ((s) * 32)) + 2048); w[2] = vtr(LP((aW0 + (so)) ^ 64) + (s) * 2048); w[3] = vtr(LP((aW0 + (so)) ^ 64) + (s) * 2048 + 512); } while (0)
#define G_MM(x, w) do { \
        acc[0][0] = MFMA32(cat8(w[0], w[1]), x[0], acc[0][0]); acc[1][0] = MFMA32(cat8(w[0], w[1]), x[1], acc[1][0]); \
        acc[0][1] = MFMA32(cat8(w[2], w[3]), x[0], acc[0][1]); acc[1][1] = MFMA32(cat8(w[2], w[3]), x[1], acc[1][1]); } while (0)
#define DBGADV pa += 1024; pb += 32 * 2048;
#define PRIO_ON
#define PRIO_OFF
#define ND ((112 - EXH) / 2)
#define PFD 6
__device__ __forceinline__ void gemm_role(const u16* __restrict__ H, const u16* __restrict__ wd, float* __restrict__ P, unsigned* __restrict__ flags, char* lds, int bid, unsigned long long* stamps)
{
    const int tid = threadIdx.x, lane = tid & 63;
    const int wid = __builtin_amdgcn_readfirstlane(tid >> 6);
    const int wm = wid & 1, wn = (wid >> 1) & 1, wk = wid >> 2;
    const int jb = bid - 256, xcd = jb & 7, idx = jb >> 3;
    const int ks = xcd & 1, pnl = idx >> 3, pml = idx & 7, pn = 4 * ((xcd >> 1) & 1) + pnl, pm = 8 * (xcd >> 2) + pml;
    const unsigned lds0 = (unsigned)(uintptr_t)lds;
    unsigned long long ts0 = 0ull, tr0 = 0ull, tA0, tA1;
    if (GEMM_PRIO) __builtin_amdgcn_s_setprio(GEMM_PRIO);
#define VW(n) asm volatile("s_waitcnt vmcnt(" #n ") lgkmcnt(0)\n\ts_barrier" ::: "memory")
    const int sr = lane >> 3, spos = lane & 7;
    const unsigned voffB = ((sr & 3) + 16 * (sr >> 2)) * 2048 + ((spos ^ (((sr >> 1) & 1) << 2)) * 16);
    const unsigned voffA = lane * 16;
    const unsigned ldsA = lds0 + wid * 1024, ldsB = lds0 + 8192 + wid * 1024;
    const char* pa = (const char*)H + ((size_t)(pm * 8 + wid) * 112 + ND * ks) * 1024;
    const char* pb = (const char*)wd + (size_t)(ND * ks * 32 + 4 * (wid & 3)) * 2048 + pn * 256 + (wid >> 2) * 128;
#define K4_ISSUE() do { const unsigned so_ = slot_i * 16384; glds16s(pa, voffA, ldsA + so_); glds16s(pb, voffB, ldsB + so_); pa += 1024; pb += 32 * 2048; slot_i = (slot_i == 4) ? 0 : slot_i + 1; } while (0)
    const int r32 = lane & 31, hi = lane >> 5, fr = lane & 15;
    const unsigned aX0 = lds0 + (wm * 4 + (r32 >> 4)) * 1024 + fr * 64 + ((hi ^ ((-(fr >> 2)) & 3)) * 16);
    const int tq = (lane & 15) >> 2, tp = lane & 3;
    const unsigned aW0 = lds0 + 8192 + wn * 4096 + (8 * hi + tq) * 128 + (((tq >> 1) & 1) << 6) + ((lane >> 4) & 1) * 32 + tp * 8;
    f32x16 acc[2][2];
#pragma unroll
    for (int m = 0; m < 2; ++m)
#pragma unroll
        for (int n = 0; n < 2; ++n) acc[m][n] = (f32x16){};
    bf16x8 xp[2] = {}, xq[2] = {}; s16x4 wp[4] = {}, wq[4] = {};
    int slot_i = 0;
#pragma unroll
    for (int i = 0; i < 5; ++i) K4_ISSUE();
    int rs_i = wk;
    VW(6);
    tA0 = 0ull;
    G_RD(xp, wp, rs_i * 16384, 0);
#define G_STEP(W, I1, I2) do { \
        const unsigned so = rs_i * 16384; \
        rs_i += 2; rs_i = (rs_i >= 5) ? rs_i - 5 : rs_i; \
        const unsigned sn = rs_i * 16384; \
        G_RD(xq, wq, so, 1); SBAR(); G_MM(xp, wp); SBAR(); \
        VW(W); \
        G_RD(xp, wp, sn, 0); SBAR(); if (I1) K4_ISSUE(); if (I2) K4_ISSUE(); SBAR(); PRIO_ON; G_MM(xq, wq); PRIO_OFF; SBAR(); } while (0)
#pragma nounroll
    for (int t = 0; t < ND / 2 - 3; ++t) { asm volatile("" : "+s"(rs_i), "+s"(slot_i)); G_STEP(2, true, true); }
    G_STEP(2, true, false);
    G_STEP(0, false, false);
    G_STEP(0, false, false);
    asm volatile("s_waitcnt lgkmcnt(0)\n\ts_barrier" ::: "memory");
    tA1 = 0ull;
    {
        int lz = threadIdx.x & 63; asm volatile("" : "+v"(lz));
        const int rr = lz & 31, hh = lz >> 5;
        f32x16 give0, give1, keep0, keep1;
        if (wk) { give0 = acc[0][0]; give1 = acc[0][1]; keep0 = acc[1][0]; keep1 = acc[1][1]; }
        else { give0 = acc[1][0]; give1 = acc[1][1]; keep0 = acc[0][0]; keep1 = acc[0][1]; }
        LAS f32x4* xs = (LAS f32x4*)(lds + ((wid & 3) * 2 + wk) * 8192) + lz;
#pragma unroll
        for (int g = 0; g < 4; ++g) {
            xs[g * 64] = (f32x4){give0[4 * g], give0[4 * g + 1], give0[4 * g + 2], give0[4 * g + 3]};
            xs[(4 + g) * 64] = (f32x4){give1[4 * g], give1[4 * g + 1], give1[4 * g + 2], give1[4 * g + 3]};
        }
        asm volatile("s_waitcnt lgkmcnt(0)\n\ts_barrier" ::: "memory");
        const LAS f32x4* xr = (const LAS f32x4*)(lds + ((wid & 3) * 2 + (1 - wk)) * 8192) + lz;
        const __amdgpu_buffer_rsrc_t rsP = __builtin_amdgcn_make_buffer_rsrc(P, 0, 8 << 20, 0x00020000);
        const unsigned pd = ((unsigned)(((ks * 16 + pm) * 8 + pn) * 8 + (wm * 2 + wk) * 2 + wn) * 256 + lz) * 16;
#pragma unroll
        for (int gp = 0; gp < 2; ++gp) {
            const int g = 2 * gp;
            const f32x4 a0 = (f32x4){keep0[4 * g], keep0[4 * g + 1], keep0[4 * g + 2], keep0[4 * g + 3]} + xr[g * 64];
            const f32x4 a1 = (f32x4){keep0[4 * g + 4], keep0[4 * g + 5], keep0[4 * g + 6], keep0[4 * g + 7]} + xr[(g + 1) * 64];
            const f32x4 b0 = (f32x4){keep1[4 * g], keep1[4 * g + 1], keep1[4 * g + 2], keep1[4 * g + 3]} + xr[(4 + g) * 64];
            const f32x4 b1 = (f32x4){keep1[4 * g + 4], keep1[4 * g + 5], keep1[4 * g + 6], keep1[4 * g + 7]} + xr[(4 + g + 1) * 64];
            const u32x4 wa = {cvtpk(a0[0], a0[1]), cvtpk(a0[2], a0[3]), cvtpk(a1[0], a1[1]), cvtpk(a1[2], a1[3])};
            const u32x4 wb = {cvtpk(b0[0], b0[1]), cvtpk(b0[2], b0[3]), cvtpk(b1[0], b1[1]), cvtpk(b1[2], b1[3])};
            __builtin_amdgcn_raw_buffer_store_b128(wa, rsP, pd + gp * 1024, 0, 16);
            __builtin_amdgcn_raw_buffer_store_b128(wb, rsP, pd + (2 + gp) * 1024, 0, 16);
        }
        asm volatile("s_waitcnt vmcnt(0)\n\ts_barrier" ::: "memory");
        if (threadIdx.x == 0) __hip_atomic_fetch_add(flags + (pm * 8 + pn) * 32, 1u, __ATOMIC_RELAXED, __HIP_MEMORY_SCOPE_AGENT);
        (void)rr; (void)hh;
    }
}

#define UNP(a, b, i) ((f32x4){__uint_as_float(a[i] << 16) + __uint_as_float(b[i] << 16), __uint_as_float(a[i] & 0xffff0000u) + __uint_as_float(b[i] & 0xffff0000u), \
                              __uint_as_float(a[(i) + 1] << 16) + __uint_as_float(b[(i) + 1] << 16), __uint_as_float(a[(i) + 1] & 0xffff0000u) + __uint_as_float(b[(i) + 1] & 0xffff0000u)})

__device__ __forceinline__ void wo_role(const u16* __restrict__ H, const u16* __restrict__ wd, const u16* __restrict__ Ob, const u16* __restrict__ wo, const float* __restrict__ P, const float* __restrict__ G, u16* __restrict__ out,
                                        unsigned* __restrict__ flags, unsigned* __restrict__ cnt, char* lds, int bid, unsigned long long* stamps)
{
    int tid = threadIdx.x; asm volatile("" : "+v"(tid));
    const int lane = tid & 63;
    const int wid = __builtin_amdgcn_readfirstlane(tid >> 6);
    const int wm = wid & 1, wn = (wid >> 1) & 1, wk = wid >> 2;
    const int xcd = bid & 7, idx = bid >> 3;
    const int rt = 8 * (xcd >> 1) + (idx & 7), pn = 4 * (xcd & 1) + (idx >> 3);
    const unsigned lds0 = (unsigned)(uintptr_t)lds;
    unsigned long long tw0 = 0ull, tw1, tw2, tw3;
    const int sr = lane >> 3, spos = lane & 7;
    const unsigned voffB = ((sr & 3) + 16 * (sr >> 2)) * 2048 + ((spos ^ (((sr >> 1) & 1) << 2)) * 16);
    const unsigned voffA = lane * 16;
    const char* pa = (const char*)H + ((size_t)(rt * 4 + (wid & 3)) * 112 + (112 - EXH) + (wid >> 2)) * 1024;
    const char* pb = (const char*)wd + (size_t)((112 - EXH) * 32 + 4 * (wid & 3)) * 2048 + pn * 256 + (wid >> 2) * 128;
    const char* paO = (const char*)Ob + ((size_t)(rt * 4 + (wid & 3)) * 32 + (wid >> 2)) * 1024;
    const char* pbO = (const char*)wo + (size_t)(4 * (wid & 3)) * 2048 + pn * 256 + (wid >> 2) * 128;
    int left = EXH / 2, sl = 0, sc = 0;
    const unsigned dA = lds0 + (wid >> 2) * 12288 + (wid & 3) * 1024, dB = lds0 + 4096 + wid * 1024;
#define K5_ISSUE() do { const unsigned so_ = sl * 24576; glds16s(pa, voffA, dA + so_); glds16s(pb, voffB, dB + so_); glds16s(pb + 32 * 2048, voffB, dB + so_ + 12288); \
                        pa += 2048; pb += (size_t)64 * 2048; sl = (sl == 2) ? 0 : sl + 1; --left; pa = (left == 0) ? paO : pa; pb = (left == 0) ? pbO : pb; } while (0)
    float gmv;
    { const unsigned go = (unsigned)(rt * 64 + wm * 32 + (lane & 31)) * 4; asm volatile("global_load_dword %0, %1, %2" : "=v"(gmv) : "v"(go), "s"(G) : "memory"); }
    unsigned touch = 0;
    K5_ISSUE(); K5_ISSUE(); K5_ISSUE();
    const int r32 = lane & 31, hi = lane >> 5, fr = lane & 15;
    const unsigned aX0 = lds0 + wk * 12288 + (wm * 2 + (r32 >> 4)) * 1024 + fr * 64 + ((hi ^ ((-(fr >> 2)) & 3)) * 16);
    const int tq = (lane & 15) >> 2, tp = lane & 3;
    const unsigned aW0 = lds0 + wk * 12288 + 4096 + wn * 4096 + (8 * hi + tq) * 128 + (((tq >> 1) & 1) << 6) + ((lane >> 4) & 1) * 32 + tp * 8;
    f32x16 acc[2] = {};
#define K5_COMP() do { const unsigned so = sc * 24576; \
        const bf16x8 x0 = ldsb128(LP(aX0 + so)), x1 = ldsb128(LP((aX0 + so) ^ 32)); \
        const s16x4 w0 = vtr(LP(aW0 + so)), w1 = vtr(LP(aW0 + so) + 512), w2 = vtr(LP((aW0 + so) ^ 64)), w3 = vtr(LP((aW0 + so) ^ 64) + 512); \
        const s16x4 w4 = vtr(LP(aW0 + so) + 2048), w5 = vtr(LP(aW0 + so) + 2560), w6 = vtr(LP((aW0 + so) ^ 64) + 2048), w7 = vtr(LP((aW0 + so) ^ 64) + 2560); \
        acc[0] = MFMA32(cat8(w0, w1), x0, acc[0]); acc[1] = MFMA32(cat8(w2, w3), x0, acc[1]); acc[0] = MFMA32(cat8(w4, w5), x1, acc[0]); acc[1] = MFMA32(cat8(w6, w7), x1, acc[1]); \
        sc = (sc == 2) ? 0 : sc + 1; } while (0)
#define K5_RUN(n_) do { _Pragma("nounroll") for (int s2 = 0; s2 < (n_); ++s2) { asm volatile("" : "+s"(sl), "+s"(sc), "+s"(left)); VW(3); K5_ISSUE(); K5_COMP(); } } while (0)
    VW(6); asm volatile("" : "+v"(gmv)); gmv = 1.0f - gmv; K5_COMP();
    K5_RUN(EXH / 2 - 3);
    if (wid == 0) {
        unsigned spins = 0;
        while (__builtin_amdgcn_readfirstlane(__hip_atomic_load(cnt + (rt >> 2) * 32, __ATOMIC_RELAXED, __HIP_MEMORY_SCOPE_AGENT)) < 32u) { __builtin_amdgcn_s_sleep(2); if (++spins > (1u << 22)) break; }
        __builtin_amdgcn_fence(__ATOMIC_ACQUIRE, "agent");
        asm volatile("s_waitcnt vmcnt(0)" ::: "memory");
    }
    tw1 = 0ull;
    K5_RUN(2);
    {
        acc[0] *= gmv; acc[1] *= gmv;
    }
    K5_RUN(13);
    if (wid == 0) {
        unsigned spins = 0;
        while (__builtin_amdgcn_readfirstlane(__hip_atomic_load(flags + ((rt >> 1) * 8 + pn) * 32, __ATOMIC_RELAXED, __HIP_MEMORY_SCOPE_AGENT)) < 2u) { __builtin_amdgcn_s_sleep(1); if (++spins > (1u << 22)) break; }
    }
    int lz = threadIdx.x & 63; asm volatile("" : "+v"(lz));
    const int rr = lz & 31, hh = lz >> 5;
    const __amdgpu_buffer_rsrc_t rsP = __builtin_amdgcn_make_buffer_rsrc((void*)P, 0, 8 << 20, 0x00020000);
    const unsigned blk = (unsigned)(((rt >> 1) * 8 + pn) * 8 + ((rt & 1) * 2 + wm) * 2 + wn);
    const unsigned p0 = (blk * 256 + wk * 128 + lz) * 16, p1 = p0 + 16 * 8 * 8 * 256 * 16;
    u32x4 pv[4];
    VW(3); K5_ISSUE();
    pv[0] = __builtin_amdgcn_raw_buffer_load_b128(rsP, p0, 0, 16); pv[1] = __builtin_amdgcn_raw_buffer_load_b128(rsP, p0 + 1024, 0, 16);
    pv[2] = __builtin_amdgcn_raw_buffer_load_b128(rsP, p1, 0, 16); pv[3] = __builtin_amdgcn_raw_buffer_load_b128(rsP, p1 + 1024, 0, 16);
    asm volatile("" ::: "memory");
    K5_COMP();
    VW(7); K5_COMP();
    VW(4); K5_COMP();
    asm volatile("" :: "v"(touch));
    tw2 = 0ull;
    f32x16 give, keep; if (wk) { give = acc[0]; keep = acc[1]; } else { give = acc[1]; keep = acc[0]; }
    asm volatile("s_waitcnt lgkmcnt(0)\n\ts_barrier" ::: "memory");
    tw3 = 0ull;
    const float gm = gmv;
    LAS f32x4* xs = (LAS f32x4*)(lds + ((wid & 3) * 2 + wk) * 4096) + lz;
#pragma unroll
    for (int g = 0; g < 4; ++g) xs[g * 64] = (f32x4){give[4 * g], give[4 * g + 1], give[4 * g + 2], give[4 * g + 3]};
    asm volatile("s_waitcnt lgkmcnt(0)\n\ts_barrier" ::: "memory");
    const LAS f32x4* xr = (const LAS f32x4*)(lds + ((wid & 3) * 2 + (1 - wk)) * 4096) + lz;
    const int row = rt * 64 + wm * 32 + rr;
#pragma unroll
    for (int g = 0; g < 4; g += 2) {
        const f32x4 v0 = (f32x4){keep[4 * g], keep[4 * g + 1], keep[4 * g + 2], keep[4 * g + 3]} + xr[g * 64] + UNP(pv[g >> 1], pv[2 + (g >> 1)], 0) * gm;
        const f32x4 v1 = (f32x4){keep[4 * g + 4], keep[4 * g + 5], keep[4 * g + 6], keep[4 * g + 7]} + xr[(g + 1) * 64] + UNP(pv[g >> 1], pv[2 + (g >> 1)], 2) * gm;
        const unsigned ax = cvtpk(v0[0], v0[1]), ay = cvtpk(v0[2], v0[3]), bx = cvtpk(v1[0], v1[1]), by = cvtpk(v1[2], v1[3]);
        auto rx = __builtin_amdgcn_permlane32_swap(ax, bx, false, false); auto ry = __builtin_amdgcn_permlane32_swap(ay, by, false, false);
        const u32x4 w = {rx[0], ry[0], rx[1], ry[1]};
        const int col = pn * 128 + wn * 64 + wk * 32 + (g + hh) * 8;
        *(u32x4*)(out + (size_t)row * 1024 + col) = w;
    }
}

__global__ __launch_bounds__(512, 4) void k23_fused(const u16* __restrict__ QKV, const float* __restrict__ KMAX, const float* __restrict__ G, u16* __restrict__ O,
                                                    const u16* __restrict__ H, const u16* __restrict__ wd, const u16* __restrict__ wo, u16* __restrict__ out, float* __restrict__ P,
                                                    unsigned* __restrict__ flags, unsigned long long* stamps)
{
    extern __shared__ __attribute__((aligned(16))) char lds[];
    const int bid = blockIdx.x;
    unsigned* cnt = flags + 256 * 32;
    if (bid < 256) { attn_role(QKV, KMAX, G, O, cnt, lds, bid, stamps); wo_role(H, wd, O, wo, P, G, out, flags, cnt, lds, bid, stamps); }
    else gemm_role(H, wd, P, flags, lds, bid, stamps);
}

extern "C" void kernel_launch(void* const* d_in, const int* in_sizes, int n_in, void* d_out, int out_size, void* d_ws, size_t ws_size, hipStream_t stream)
{
    const u16* x = (const u16*)d_in[0]; const u16* wqkv = (const u16*)d_in[1]; const u16* wo = (const u16*)d_in[2];
    const u16* wg = (const u16*)d_in[4]; const u16* wu = (const u16*)d_in[5]; const u16* wd = (const u16*)d_in[6]; const u16* gw = (const u16*)d_in[7];
    char* ws = (char*)d_ws;
    u16* QKV = (u16*)ws;
    u16* H = (u16*)(ws + (16u << 20));
    u16* Ob = (u16*)(ws + (32u << 20));
    float* P = (float*)(ws + (40u << 20));
    float* G = (float*)(ws + (48u << 20));
    float* KM = (float*)(ws + (48u << 20) + 65536);
    unsigned* flags = (unsigned*)(ws + (48u << 20) + 131072);
    hipFuncSetAttribute((const void*)k1_gemm, hipFuncAttributeMaxDynamicSharedMemorySize, K1_LDS);
    hipFuncSetAttribute((const void*)k23_fused, hipFuncAttributeMaxDynamicSharedMemorySize, K23_LDS);
    hipLaunchKernelGGL(k1_gemm, dim3(256), dim3(512), K1_LDS, stream, x, wqkv, wg, wu, gw, QKV, H, G, KM, flags, wd, wo, (unsigned long long*)nullptr);
    hipLaunchKernelGGL(k23_fused, dim3(512), dim3(512), K23_LDS, stream, QKV, KM, G, Ob, H, wd, wo, (u16*)d_out, P, flags, (unsigned long long*)nullptr);
}
```

```cpp
#define K1_AUX 2
#define EXH 8
#include <hip/hip_runtime.h>
#include <stdint.h>
typedef unsigned short u16;
typedef short bf16x8 __attribute__((ext_vector_type(8)));
typedef short s16x4 __attribute__((ext_vector_type(4)));
typedef float f32x4 __attribute__((ext_vector_type(4)));
typedef float f32x16 __attribute__((ext_vector_type(16)));
typedef unsigned u32x4 __attribute__((ext_vector_type(4)));
#define LAS __attribute__((address_space(3)))
typedef LAS const char* lds_cptr;
typedef short v4i16_t __attribute__((ext_vector_type(4)));
#define SBAR() __builtin_amdgcn_sched_barrier(0)

constexpr int S_ = 2048, D_ = 1024, F_ = 3584, NH_ = 32;
constexpr float QSCALE = 0.17677669529663687f * 1.4426950408889634f;

typedef __bf16 bf16x2_t __attribute__((ext_vector_type(2)));
typedef float f32x2_t __attribute__((ext_vector_type(2)));
__device__ __forceinline__ unsigned cvtpk(float lo, float hi) { const f32x2_t v = {lo, hi}; return __builtin_bit_cast(unsigned, __builtin_convertvector(v, bf16x2_t)); }
__device__ __forceinline__ void glds16s(const void* sbase, unsigned voff, unsigned lds_base) {
    asm volatile("s_mov_b32 m0, %2\n\ts_nop 0\n\tglobal_load_lds_dwordx4 %0, %1"
                 :: "v"(voff), "s"(sbase), "s"(lds_base) : "memory");
}
__device__ __forceinline__ s16x4 vtr(lds_cptr p) { return __builtin_bit_cast(s16x4, __builtin_amdgcn_ds_read_tr16_b64_v4i16((LAS v4i16_t*)p)); }
__device__ __forceinline__ bf16x8 cat8(s16x4 a, s16x4 b) { return (bf16x8){a[0], a[1], a[2], a[3], b[0], b[1], b[2], b[3]}; }
__device__ __forceinline__ bf16x8 ldsb128(lds_cptr p) { return *(const LAS bf16x8*)p; }

__device__ __forceinline__ lds_cptr LP(unsigned a) { return (lds_cptr)(uintptr_t)a; }
template <int X> __device__ __forceinline__ float swz_xor(float v) { return __int_as_float(__builtin_amdgcn_ds_swizzle(__float_as_int(v), (X << 10) | 0x1f)); }
__device__ __forceinline__ float add32(float v) { auto r = __builtin_amdgcn_permlane32_swap(__float_as_uint(v), __float_as_uint(v), false, false); return __uint_as_float(r[0]) + __uint_as_float(r[1]); }
__device__ __forceinline__ float max32(float v) { auto r = __builtin_amdgcn_permlane32_swap(__float_as_uint(v), __float_as_uint(v), false, false); return fmaxf(__uint_as_float(r[0]), __uint_as_float(r[1])); }
constexpr int K1_STAGE = 73728, K1_LDS = 2 * K1_STAGE;

__device__ __forceinline__ float gelu_mul(float g, float u) {
    const float t = g * g;
    const float p = __builtin_fmaf(t, -0.1029432397f, -2.302208198f);
    const float e = __builtin_amdgcn_exp2f(g * p);
    const float r = __builtin_amdgcn_rcpf(1.0f + e);
    return g * u * r;
}

__device__ __forceinline__ unsigned gelu_mul2(float g0, float g1, float u0, float u1) {
    const f32x2_t g = {g0, g1}, u = {u0, u1};
    const f32x2_t t = g * g;
    const f32x2_t p = t * -0.1029432397f + -2.302208198f;
    const f32x2_t z = g * p;
    f32x2_t e; e[0] = __builtin_amdgcn_exp2f(z[0]); e[1] = __builtin_amdgcn_exp2f(z[1]);
    const f32x2_t d = e + 1.0f;
    f32x2_t r; r[0] = __builtin_amdgcn_rcpf(d[0]); r[1] = __builtin_amdgcn_rcpf(d[1]);
    const f32x2_t h = (g * u) * r;
    return __builtin_bit_cast(unsigned, __builtin_convertvector(h, bf16x2_t));
}

__global__ __launch_bounds__(512, 2) void k1_gemm(const u16* __restrict__ x, const u16* __restrict__ wqkv, const u16* __restrict__ wg, const u16* __restrict__ wu,
                                                  const u16* __restrict__ gw, u16* __restrict__ QKV, u16* __restrict__ H, float* __restrict__ G, float* __restrict__ KMAX, unsigned* __restrict__ flags, const u16* __restrict__ wd, const u16* __restrict__ wo)
{
    extern __shared__ __attribute__((aligned(16))) char lds[];
    const int tid = threadIdx.x, lane = tid & 63;
    const int wid = __builtin_amdgcn_readfirstlane(tid >> 6);
    const int wm = wid & 3, wn = wid >> 2;
    const int fr = lane & 15, fq = lane >> 4;
    const int bid = blockIdx.x, xcd = bid & 7, idx = bid >> 3, pm = idx & 7, pn = xcd * 4 + (idx >> 3);
    const bool typeB = pn >= 24;
    const unsigned lds0 = (unsigned)(uintptr_t)lds;

    if (bid == 0) { for (int i = tid; i < 264 * 32; i += 512) flags[i] = 0u; }
    const char* cb0; const char* cb1; const char* cb2; const char* cb3; const char* cb4;
    unsigned ldmid;
    if (!typeB) { cb0 = (const char*)(wg + 128 * pn); cb1 = (const char*)(wu + 128 * pn); cb2 = cb0 + 128; cb3 = cb1 + 128; cb4 = (const char*)(wqkv + 64 * pn); ldmid = F_ * 2; }
    else { const int i = pn - 24; cb0 = (const char*)(wg + 3072 + 64 * i); cb1 = (const char*)(wu + 3072 + 64 * i); cb2 = (const char*)(wqkv + 64 * (24 + 3 * i)); cb3 = cb2 + 128; cb4 = cb2 + 256; ldmid = 3072 * 2; }
    cb0 += (size_t)(8 * wid) * (F_ * 2); cb1 += (size_t)(8 * wid) * (F_ * 2); cb2 += (size_t)(8 * wid) * ldmid; cb3 += (size_t)(8 * wid) * ldmid; cb4 += (size_t)(8 * wid) * (3072 * 2);
    const char* ca = (const char*)x + (size_t)(256 * pm + 8 * wid) * 2048;
    const int sr = lane >> 3, spos = lane & 7;
    const unsigned fB = ((sr >> 1) & 1) | ((wid & 1) << 1);
    const unsigned colB = (((spos >> 1) ^ fB) * 32) + (spos & 1) * 16;
    const unsigned voffG = sr * (F_ * 2) + colB, voffM = sr * ldmid + colB, voffQ = sr * (3072 * 2) + colB;
    const unsigned voffA = sr * 2048 + ((spos ^ (4 * (wid & 1) + (sr >> 1))) * 16);
    const unsigned ldsw = lds0 + wid * 1024;
    float gsig;


#define K1_PIECE(i, sb) do { \
        if ((i) < 4) glds16s(ca + (size_t)(i) * 131072, voffA, (sb) + (i) * 8192); \
        else if ((i) == 4) glds16s(cb0, voffG, (sb) + 32768); \
        else if ((i) == 5) glds16s(cb2, voffM, (sb) + 32768 + 16384); \
        else if ((i) == 6) glds16s(cb1, voffG, (sb) + 32768 + 8192); \
        else if ((i) == 7) glds16s(cb3, voffM, (sb) + 32768 + 24576); \
        else glds16s(cb4, voffQ, (sb) + 32768 + 32768); } while (0)
#define K1_ADV1() do { ca += 128; cb0 += 64 * F_ * 2; cb2 += (size_t)64 * ldmid; } while (0)
#define K1_ADV2() do { cb1 += 64 * F_ * 2; cb3 += (size_t)64 * ldmid; cb4 += 64 * 3072 * 2; } while (0)

    const int grow = 256 * pm + 8 * pn + wid;
    u32x4 gx0, gx1, gg0, gg1;
    {
        const char* xr = (const char*)(x + (size_t)grow * D_);
        const unsigned go = lane * 32;
        asm volatile("global_load_dwordx4 %0, %4, %5\n\tglobal_load_dwordx4 %1, %4, %5 offset:16\n\tglobal_load_dwordx4 %2, %4, %6\n\tglobal_load_dwordx4 %3, %4, %6 offset:16"
                     : "=&v"(gx0), "=&v"(gx1), "=&v"(gg0), "=&v"(gg1) : "v"(go), "s"(xr), "s"(gw) : "memory");
    }
#pragma unroll
    for (int i = 0; i < 9; ++i) K1_PIECE(i, ldsw);
    K1_ADV1(); K1_ADV2();
#pragma unroll
    for (int i = 0; i < 6; ++i) K1_PIECE(i, ldsw + K1_STAGE);
    K1_ADV1();
#define GWAIT 15
#define STR2(x) #x
#define STR(x) STR2(x)
    asm volatile("s_waitcnt vmcnt(" STR(GWAIT) ")" : "+v"(gx0), "+v"(gx1), "+v"(gg0), "+v"(gg1) :: "memory");
    {
        float s = 0.f;
#pragma unroll
        for (int j = 0; j < 4; ++j) {
            s += __uint_as_float(gx0[j] << 16) * __uint_as_float(gg0[j] << 16) + __uint_as_float(gx0[j] & 0xffff0000u) * __uint_as_float(gg0[j] & 0xffff0000u);
            s += __uint_as_float(gx1[j] << 16) * __uint_as_float(gg1[j] << 16) + __uint_as_float(gx1[j] & 0xffff0000u) * __uint_as_float(gg1[j] & 0xffff0000u);
        }
        s += swz_xor<1>(s); s += swz_xor<2>(s); s += swz_xor<4>(s); s += swz_xor<8>(s); s += swz_xor<16>(s); s = add32(s);
        gsig = 1.0f / (1.0f + __expf(-s));
    }
    unsigned aA0 = lds0 + (64 * wm + fr) * 128 + (((0 + fq) ^ (fr >> 1)) * 16);
    unsigned aA1 = lds0 + (64 * wm + fr) * 128 + (((4 + fq) ^ (fr >> 1)) * 16);
    const int tq = (lane & 15) >> 2, tp = lane & 3;
    const unsigned fR = ((tq >> 1) & 1) | ((fq & 1) << 1);
    const unsigned bbase = lds0 + 32768 + (8 * fq + tq) * 128 + 8 * tp;
    unsigned aB0 = bbase + 2 * wn * 8192 + ((0 ^ fR) << 5);
    unsigned aB1 = bbase + 2 * wn * 8192 + ((1 ^ fR) << 5);
    unsigned aB2 = bbase + 2 * wn * 8192 + ((2 ^ fR) << 5);
    unsigned aB3 = bbase + 2 * wn * 8192 + ((3 ^ fR) << 5);
    unsigned aB4a = bbase + 4 * 8192 + (((2 * wn) ^ fR) << 5);
    unsigned aB4b = bbase + 4 * 8192 + (((2 * wn + 1) ^ fR) << 5);

    f32x4 acc[4][10];
#pragma unroll
    for (int m = 0; m < 4; ++m)
#pragma unroll
        for (int s = 0; s < 10; ++s) acc[m][s] = (f32x4){0.f, 0.f, 0.f, 0.f};
    bf16x8 fa0[4], fa1[4];
    s16x4 bx[4], by[4];

#define MF(fb, h, fa, m, ti) acc[m][(ti) + (h)] = __builtin_amdgcn_mfma_f32_16x16x32_bf16(cat8(fb[2 * (h)], fb[2 * (h) + 1]), fa[m], acc[m][(ti) + (h)], 0, 0, 0)
#define RB(dst, j, aa, ab, off) dst[j] = vtr(LP(((j) < 2) ? (aa) : (ab)) + (off) + ((j) & 1) * 512)
#define RA(dst, m, a) dst[m] = ldsb128(LP(a) + (m) * 2048)
#define SUBP(cur, fa, ti, nxt, aa, ab, off, AR, GL) do { \
        MF(cur, 0, fa, 0, ti); SBAR(); RB(nxt, 0, aa, ab, off); SBAR(); \
        MF(cur, 1, fa, 0, ti); SBAR(); RB(nxt, 1, aa, ab, off); SBAR(); \
        MF(cur, 0, fa, 1, ti); SBAR(); RB(nxt, 2, aa, ab, off); SBAR(); \
        MF(cur, 1, fa, 1, ti); SBAR(); RB(nxt, 3, aa, ab, off); SBAR(); \
        MF(cur, 0, fa, 2, ti); SBAR(); AR(0); GL; SBAR(); \
        MF(cur, 1, fa, 2, ti); SBAR(); AR(1); SBAR(); \
        MF(cur, 0, fa, 3, ti); SBAR(); AR(2); SBAR(); \
        MF(cur, 1, fa, 3, ti); SBAR(); AR(3); SBAR(); } while (0)
#define NOA(m) do { } while (0)
#define RA0(m) RA(fa0, m, aA0)
#define RA1(m) RA(fa1, m, aA1)
#define NOG do { } while (0)
#define WBAR(n) asm volatile("s_waitcnt vmcnt(" #n ") lgkmcnt(0)\n\ts_barrier" ::: "memory")

    unsigned warm0 = 0;
    const int wgi = bid * 8 + wid;
    const char* wp = wgi < 1275 ? (const char*)wd + min(wgi * 5760, 7340032 - 5760) : (const char*)wo + min((wgi - 1275) * 5760, 2097152 - 5760);
    const unsigned wvo = (lane % 6) * 64;
    asm volatile("global_load_dword %0, %1, %2" : "+v"(warm0) : "v"(wvo), "s"(wp) : "memory"); wp += 384;
    K1_PIECE(6, ldsw + K1_STAGE);
    asm volatile("s_waitcnt vmcnt(11)\n\ts_barrier" ::: "memory");
    if (lane == 0) G[grow] = gsig;
#pragma unroll
    for (int j = 0; j < 4; ++j) RB(bx, j, aB0, aB1, 0);
#pragma unroll
    for (int m = 0; m < 4; ++m) RA0(m);
    int delta = K1_STAGE;

#define WBARM() WBAR(10)
#define VADD(a) asm volatile("v_add_u32 %0, %1, %0" : "+v"(a) : "s"(delta))
#define STEP(MAIN) do { \
        SUBP(bx, fa0, 0, by, aB0, aB1, 4096, RA1, if (MAIN || p2) K1_PIECE(7, ns)); \
        SUBP(by, fa1, 0, bx, aB2, aB3, 0, NOA, if (MAIN || p2) { K1_PIECE(8, ns); K1_ADV2(); }); \
        SUBP(bx, fa0, 2, by, aB2, aB3, 4096, NOA, NOG); \
        if (MAIN) WBARM(); else if (p2) WBAR(9); else WBAR(0); \
        SUBP(by, fa1, 2, bx, aB0, aB1, 8192, NOA, if (MAIN) K1_PIECE(0, cs)); \
        SUBP(bx, fa0, 4, by, aB0, aB1, 8192 + 4096, NOA, if (MAIN) K1_PIECE(1, cs)); \
        SUBP(by, fa1, 4, bx, aB2, aB3, 8192, NOA, if (MAIN) K1_PIECE(2, cs)); \
        SUBP(bx, fa0, 6, by, aB2, aB3, 8192 + 4096, NOA, if (MAIN) K1_PIECE(3, cs)); \
        SUBP(by, fa1, 6, bx, aB4a, aB4b, 0, NOA, if (MAIN) K1_PIECE(4, cs)); \
        SUBP(bx, fa0, 8, by, aB4a, aB4b, 4096, NOA, if (MAIN) { K1_PIECE(5, cs); K1_ADV1(); }); \
        VADD(aA0); VADD(aA1); VADD(aB0); VADD(aB1); VADD(aB2); VADD(aB3); \
        if (MAIN) WBARM(); else WBAR(3); \
        SUBP(by, fa1, 8, bx, aB0, aB1, 0, RA0, if (MAIN) K1_PIECE(6, cs)); \
        VADD(aB4a); VADD(aB4b); delta = -delta; { const unsigned tmp = cs; cs = ns; ns = tmp; } \
    } while (0)
    unsigned cs = ldsw, ns = ldsw + K1_STAGE;
#pragma nounroll
    for (int tt = 0; tt < 14; ++tt) {
        const bool p2 = true; STEP(true); asm volatile("" : "+s"(cs), "+s"(ns), "+s"(delta));
        asm volatile("global_load_dword %0, %1, %2" : "+v"(warm0) : "v"(wvo), "s"(wp) : "memory"); wp += 384;
    }
#pragma nounroll
    for (int tt = 0; tt < 2; ++tt) { int t = tt; asm volatile("" : "+s"(t)); const bool p2 = t == 0; STEP(false); asm volatile("" : "+s"(cs), "+s"(ns), "+s"(delta)); }

    const int row0 = 256 * pm + 64 * wm;
    const __amdgpu_buffer_rsrc_t rsH = __builtin_amdgcn_make_buffer_rsrc(H, 0, 2048 * 3584 * 2, 0x00020000), rsQ = __builtin_amdgcn_make_buffer_rsrc(QKV, 0, 96 * 2048 * 64, 0x00020000);
#define ST16(base, rs, off, w) __builtin_amdgcn_raw_buffer_store_b128(w, rs, off, 0, K1_AUX)
    const unsigned swz = ((fq ^ ((-(fr >> 2)) & 3)) * 16) + fr * 64;
    const bool gu = !typeB || wn == 0;
    if (gu) {
        const int hc0 = typeB ? (3072 + 64 * (pn - 24)) : (128 * pn + 64 * wn);
#pragma unroll
        for (int m = 0; m < 4; ++m) {
            const int mt = (row0 >> 4) + m;
#pragma unroll
            for (int i = 0; i < 2; ++i) {
                u32x4 w;
                { const f32x4 g = acc[m][2 * i], u = acc[m][4 + 2 * i];
                  w[0] = gelu_mul2(g[0], g[1], u[0], u[1]); w[1] = gelu_mul2(g[2], g[3], u[2], u[3]); }
                { const f32x4 g = acc[m][2 * i + 1], u = acc[m][4 + 2 * i + 1];
                  w[2] = gelu_mul2(g[0], g[1], u[0], u[1]); w[3] = gelu_mul2(g[2], g[3], u[2], u[3]); }
                ST16(H, rsH, ((unsigned)mt * 112 + (hc0 >> 5) + i) * 1024 + swz, w);
            }
        }
    }
    auto store_head = [&](int hb, const f32x4 (&ta)[4], const f32x4 (&tb)[4]) {
        const int which = hb >> 5;
        const float sc = which == 0 ? QSCALE : 1.0f;
        float kn = 0.f;
#pragma unroll
        for (int m = 0; m < 4; ++m) {
            const f32x4 a = ta[m] * sc, b = tb[m] * sc;
            u32x4 w; w[0] = cvtpk(a[0], a[1]); w[1] = cvtpk(a[2], a[3]); w[2] = cvtpk(b[0], b[1]); w[3] = cvtpk(b[2], b[3]);
            ST16(QKV, rsQ, ((unsigned)hb * 2048 + row0 + 16 * m + fr) * 64 + fq * 16, w);
            if (which == 1) {
                float n2 = a[0] * a[0] + a[1] * a[1] + a[2] * a[2] + a[3] * a[3] + b[0] * b[0] + b[1] * b[1] + b[2] * b[2] + b[3] * b[3];
                n2 += swz_xor<16>(n2); n2 = add32(n2);
                kn = fmaxf(kn, n2);
            }
        }
        if (which == 1) {
            kn = fmaxf(kn, swz_xor<8>(kn)); kn = fmaxf(kn, swz_xor<4>(kn)); kn = fmaxf(kn, swz_xor<2>(kn)); kn = fmaxf(kn, swz_xor<1>(kn));
            if (lane == 0) KMAX[(hb & 31) * 32 + pm * 4 + wm] = kn;
        }
    };
    {
        const int qc4 = typeB ? (24 + 3 * (pn - 24) + 2) : pn;
        const f32x4 ta[4] = {acc[0][8], acc[1][8], acc[2][8], acc[3][8]};
        const f32x4 tb[4] = {acc[0][9], acc[1][9], acc[2][9], acc[3][9]};
        store_head(2 * qc4 + wn, ta, tb);
    }
    if (!gu) {
        const int qc2 = 24 + 3 * (pn - 24);
#pragma unroll
        for (int c = 0; c < 2; ++c)
#pragma unroll
            for (int hh = 0; hh < 2; ++hh) {
                const f32x4 ta[4] = {acc[0][4 * c + 2 * hh], acc[1][4 * c + 2 * hh], acc[2][4 * c + 2 * hh], acc[3][4 * c + 2 * hh]};
                const f32x4 tb[4] = {acc[0][4 * c + 2 * hh + 1], acc[1][4 * c + 2 * hh + 1], acc[2][4 * c + 2 * hh + 1], acc[3][4 * c + 2 * hh + 1]};
                store_head(2 * (qc2 + c) + hh, ta, tb);
            }
    }
    asm volatile("" :: "v"(warm0));
}

#define MFMA32(a, b, c) __builtin_amdgcn_mfma_f32_32x32x16_bf16(a, b, c, 0, 0, 0)
#define MFMA16(a, b, c) __builtin_amdgcn_mfma_f32_16x16x32_bf16(a, b, c, 0, 0, 0)
constexpr int K2_SLOT = 16384, K2_NS = 4, K2_LDS = K2_SLOT * K2_NS;

__device__ __forceinline__ bf16x8 pack8(const f32x16& p, int b) {
    u32x4 w; w[0] = cvtpk(p[b], p[b + 1]); w[1] = cvtpk(p[b + 2], p[b + 3]); w[2] = cvtpk(p[b + 4], p[b + 5]); w[3] = cvtpk(p[b + 6], p[b + 7]);
    return __builtin_bit_cast(bf16x8, w);
}


#define K2_ISSUE(st) do { glds16s(Kh + (size_t)(st) * 8192, voffK, ldsw + ((st) & 3) * K2_SLOT); glds16s(Vh + (size_t)(st) * 8192, voffV, ldsw + ((st) & 3) * K2_SLOT + 8192); } while (0)
#define RDK(d0, d1, slot, i) do { d0 = ldsb128(LP(aK0 + (slot)) + (i) * 2048); d1 = ldsb128(LP(aK1 + (slot)) + (i) * 2048); } while (0)
#define QK(S, k0, k1) do { S = MFMA32(k0, qf0, negm); S = MFMA32(k1, qf1, S); } while (0)
#define PIN(x) asm volatile("" : "+v"(x))
#define SUM0 lsum = MFMA16(sel, p0, lsum)
#define SUM1 lsum2 = MFMA16(sel, p1, lsum2)
#define EX4(S, b) do { S[b] = __builtin_amdgcn_exp2f(S[b]); S[b + 1] = __builtin_amdgcn_exp2f(S[b + 1]); S[b + 2] = __builtin_amdgcn_exp2f(S[b + 2]); S[b + 3] = __builtin_amdgcn_exp2f(S[b + 3]); } while (0)
#define RDV(d, slot, i) do { d[0] = vtr(LP(aV + (slot)) + (i) * 2048); d[1] = vtr(LP(aV + (slot)) + (i) * 2048 + 512); d[2] = vtr(LP(aV + (slot)) + (i) * 2048 + 1024); d[3] = vtr(LP(aV + (slot)) + (i) * 2048 + 1536); } while (0)
#define ITER(SC, SN, k0, k1, n0, n1, vc, vn, kslot, ki, vslot, vi) do { \
        SN = MFMA32(k0, qf0, negm); SBAR(); \
        EX4(SC, 0); PIN(SC); SBAR(); \
        SN = MFMA32(k1, qf1, SN); SBAR(); \
        RDK(n0, n1, kslot, ki); SBAR(); \
        EX4(SC, 4); const bf16x8 p0 = pack8(SC, 0); SBAR(); \
        o = MFMA32(cat8(vc[0], vc[1]), p0, o); SBAR(); \
        EX4(SC, 8); PIN(SC); SBAR(); \
        SUM0; SBAR(); \
        EX4(SC, 12); const bf16x8 p1 = pack8(SC, 8); SBAR(); \
        o = MFMA32(cat8(vc[2], vc[3]), p1, o); SBAR(); \
        RDV(vn, vslot, vi); SBAR(); \
        SUM1; SBAR(); \
    } while (0)
#define WB2(n) asm volatile("s_waitcnt vmcnt(" #n ") lgkmcnt(0)\n\ts_barrier" ::: "memory")
constexpr int K23_LDS = 81920;

__device__ __forceinline__ void attn_role(const u16* __restrict__ QKV, const float* __restrict__ KMAX, const float* __restrict__ G, u16* __restrict__ O, unsigned* __restrict__ cnt, char* lds, int bid)
{
    const int tid = threadIdx.x, lane = tid & 63;
    const int wid = __builtin_amdgcn_readfirstlane(tid >> 6);
    const int r32 = lane & 31, hi = lane >> 5;
    const int xcd = bid & 7, idx = bid >> 3, head = 4 * xcd + (idx >> 3), qb = idx & 7;
    const unsigned lds0 = (unsigned)(uintptr_t)lds;
    __builtin_amdgcn_s_setprio(1);
    const char* Qh = (const char*)QKV + (size_t)head * 131072;
    const char* Kh = (const char*)QKV + (size_t)(32 + head) * 131072 + wid * 1024;
    const char* Vh = (const char*)QKV + (size_t)(64 + head) * 131072 + wid * 1024;
    const unsigned voffK = (lane >> 2) * 64 + (((lane & 3) ^ ((lane >> 4) & 3)) * 16), voffV = lane * 16;
    const unsigned ldsw = lds0 + wid * 1024;
    K2_ISSUE(0); K2_ISSUE(1);
    const int q0 = qb * 256 + wid * 32;
    const bf16x8 qf0 = *(const bf16x8*)(Qh + (size_t)(q0 + r32) * 64 + hi * 16);
    const bf16x8 qf1 = *(const bf16x8*)(Qh + (size_t)(q0 + r32) * 64 + 32 + hi * 16);
    const float kmx = KMAX[head * 32 + r32];
    float gq = G[q0 + r32];
    float qn2 = 0.f;
#pragma unroll
    for (int j = 0; j < 8; ++j) { const float a = __uint_as_float((unsigned)(u16)qf0[j] << 16), b = __uint_as_float((unsigned)(u16)qf1[j] << 16); qn2 += a * a + b * b; }
    qn2 = add32(qn2);
    float km2 = kmx; km2 = fmaxf(km2, swz_xor<1>(km2)); km2 = fmaxf(km2, swz_xor<2>(km2)); km2 = fmaxf(km2, swz_xor<4>(km2)); km2 = fmaxf(km2, swz_xor<8>(km2)); km2 = fmaxf(km2, swz_xor<16>(km2));
    const float mb = sqrtf(qn2 * km2) * 1.01f;
    f32x16 negm;
#pragma unroll
    for (int r = 0; r < 16; ++r) negm[r] = -mb;
    asm volatile("" : "+v"(negm), "+v"(gq));
    bf16x8 sel;
    { const short one = ((lane & 15) == ((lane >> 4) & 1)) ? (short)0x3F80 : (short)0;
#pragma unroll
      for (int j = 0; j < 8; ++j) sel[j] = one; }
    const unsigned aK0 = lds0 + r32 * 64 + (((0 + hi) ^ ((r32 >> 2) & 3)) * 16);
    const unsigned aK1 = lds0 + r32 * 64 + (((2 + hi) ^ ((r32 >> 2) & 3)) * 16);
    const unsigned aV = lds0 + 8192 + (4 * hi + ((lane & 15) >> 2)) * 64 + ((lane >> 4) & 1) * 32 + (lane & 3) * 8;
    f32x16 o = {}; f32x4 lsum = {0.f, 0.f, 0.f, 0.f}, lsum2 = {0.f, 0.f, 0.f, 0.f};
    f32x16 sA, sB;
    bf16x8 kfa0, kfa1, kfb0, kfb1;
    s16x4 va[4], vb[4];
    WB2(2);
    K2_ISSUE(2);
    RDK(kfa0, kfa1, 0, 0); RDK(kfb0, kfb1, 0, 1); RDV(va, 0, 0);
    QK(sA, kfa0, kfa1);
#define ATT_STAGE(MAIN) do { \
        const unsigned cur = (stv & 3) * K2_SLOT, nxt = ((stv + 1) & 3) * K2_SLOT; \
        ITER(sA, sB, kfb0, kfb1, kfa0, kfa1, va, vb, cur, 2, cur, 1); \
        ITER(sB, sA, kfa0, kfa1, kfb0, kfb1, vb, va, cur, 3, cur, 2); \
        if (MAIN) { WB2(2); K2_ISSUE(stv + 3); } else WB2(0); \
        ITER(sA, sB, kfb0, kfb1, kfa0, kfa1, va, vb, nxt, 0, cur, 3); \
        ITER(sB, sA, kfa0, kfa1, kfb0, kfb1, vb, va, nxt, 1, nxt, 0); } while (0)
#pragma nounroll
    for (int st = 0; st < 13; ++st) { int stv = st; asm volatile("" : "+s"(stv)); ATT_STAGE(true); }
#pragma nounroll
    for (int st = 13; st < 16; ++st) { int stv = st; asm volatile("" : "+s"(stv)); ATT_STAGE(false); }
    lsum += lsum2;
    int lz = threadIdx.x & 63; asm volatile("" : "+v"(lz));
    const int rr = lz & 31, hh = lz >> 5;
    float l;
    {
        const float l0 = __int_as_float(__builtin_amdgcn_ds_bpermute((rr & 15) << 2, __float_as_int(lsum[0]))), l1 = __int_as_float(__builtin_amdgcn_ds_bpermute((rr & 15) << 2, __float_as_int(lsum[1])));
        l = (rr & 16) ? l1 : l0;
    }
    const bool redo = __any(!(l >= 3.23117427e-27f));
    if (redo) {
        const char* Kg = (const char*)QKV + (size_t)(32 + head) * 131072 + rr * 64 + hh * 16;
        const u16* Vg = (const u16*)((const char*)QKV + (size_t)(64 + head) * 131072) + rr;
        const f32x16 zero = {};
        float m = -INFINITY;
#pragma nounroll
        for (int t = 0; t < 64; ++t) {
            const bf16x8 k0 = *(const bf16x8*)(Kg + (size_t)t * 2048), k1 = *(const bf16x8*)(Kg + (size_t)t * 2048 + 32);
            f32x16 sc2 = MFMA32(k0, qf0, zero); sc2 = MFMA32(k1, qf1, sc2);
#pragma unroll
            for (int r = 0; r < 16; ++r) m = fmaxf(m, sc2[r]);
        }
        m = max32(m);
        f32x16 nm;
#pragma unroll
        for (int r = 0; r < 16; ++r) nm[r] = -m;
        o = zero; float ls = 0.f;
#pragma nounroll
        for (int t = 0; t < 64; ++t) {
            const bf16x8 k0 = *(const bf16x8*)(Kg + (size_t)t * 2048), k1 = *(const bf16x8*)(Kg + (size_t)t * 2048 + 32);
            f32x16 sc2 = MFMA32(k0, qf0, nm); sc2 = MFMA32(k1, qf1, sc2);
#pragma unroll
            for (int r = 0; r < 16; ++r) sc2[r] = __builtin_amdgcn_exp2f(sc2[r]);
            const bf16x8 p0 = pack8(sc2, 0), p1 = pack8(sc2, 8);
            bf16x8 v0, v1;
#pragma unroll
            for (int j = 0; j < 8; ++j) {
                const int key = t * 32 + 8 * (j >> 2) + 4 * hh + (j & 3);
                v0[j] = (short)Vg[(size_t)key * 32]; v1[j] = (short)Vg[(size_t)(key + 16) * 32];
                ls += __uint_as_float((unsigned)(u16)p0[j] << 16) + __uint_as_float((unsigned)(u16)p1[j] << 16);
            }
            o = MFMA32(v0, p0, o); o = MFMA32(v1, p1, o);
        }
        l = add32(ls);
    }
    const float sc = gq / l;
    const int row = q0 + rr, fr = row & 15, mt = row >> 4;
    const __amdgpu_buffer_rsrc_t rs = __builtin_amdgcn_make_buffer_rsrc(O, 0, 2048 * 1024 * 2, 0x00020000);
    const unsigned ob = (unsigned)(mt * 32 + head) * 1024 + fr * 64;
    const int gsw = (-(fr >> 2)) & 3;
#pragma unroll
    for (int c = 0; c < 4; c += 2) {
        unsigned ax = cvtpk(o[4 * c] * sc, o[4 * c + 1] * sc), ay = cvtpk(o[4 * c + 2] * sc, o[4 * c + 3] * sc);
        unsigned bx = cvtpk(o[4 * c + 4] * sc, o[4 * c + 5] * sc), by = cvtpk(o[4 * c + 6] * sc, o[4 * c + 7] * sc);
        auto rx = __builtin_amdgcn_permlane32_swap(ax, bx, false, false); auto ry = __builtin_amdgcn_permlane32_swap(ay, by, false, false);
        const u32x4 w = {rx[0], ry[0], rx[1], ry[1]};
        __builtin_amdgcn_raw_buffer_store_b128(w, rs, ob + (((c + hh) ^ gsw) * 16), 0, 16);
    }
    asm volatile("s_waitcnt vmcnt(0) lgkmcnt(0)\n\ts_barrier" ::: "memory");
    if (threadIdx.x == 0) __hip_atomic_fetch_add(cnt + qb * 32, 1u, __ATOMIC_RELAXED, __HIP_MEMORY_SCOPE_AGENT);
}

#define G_RD(x, w, so, s) do { \
        x[0] = ldsb128(LP((aX0 + (so)) ^ ((s) * 32))); w[0] = vtr(LP(aW0 + (so)) + (s) * 2048); w[1] = vtr(LP(aW0 + (so)) + (s) * 2048 + 512); \
        x[1] = ldsb128(LP((aX0 + (so)) ^ ((s) * 32)) + 2048); w[2] = vtr(LP((aW0 + (so)) ^ 64) + (s) * 2048); w[3] = vtr(LP((aW0 + (so)) ^ 64) + (s) * 2048 + 512); } while (0)
#define G_MM(x, w) do { \
        acc[0][0] = MFMA32(cat8(w[0], w[1]), x[0], acc[0][0]); acc[1][0] = MFMA32(cat8(w[0], w[1]), x[1], acc[1][0]); \
        acc[0][1] = MFMA32(cat8(w[2], w[3]), x[0], acc[0][1]); acc[1][1] = MFMA32(cat8(w[2], w[3]), x[1], acc[1][1]); } while (0)
#define DBGADV pa += 1024; pb += 32 * 2048;
#define ND ((112 - EXH) / 2)
__device__ __forceinline__ void gemm_role(const u16* __restrict__ H, const u16* __restrict__ wd, float* __restrict__ P, unsigned* __restrict__ flags, char* lds, int bid)
{
    const int tid = threadIdx.x, lane = tid & 63;
    const int wid = __builtin_amdgcn_readfirstlane(tid >> 6);
    const int wm = wid & 1, wn = (wid >> 1) & 1, wk = wid >> 2;
    const int jb = bid - 256, xcd = jb & 7, idx = jb >> 3;
    const int ks = xcd & 1, pnl = idx >> 3, pml = idx & 7, pn = 4 * ((xcd >> 1) & 1) + pnl, pm = 8 * (xcd >> 2) + pml;
    const unsigned lds0 = (unsigned)(uintptr_t)lds;
#define VW(n) asm volatile("s_waitcnt vmcnt(" #n ") lgkmcnt(0)\n\ts_barrier" ::: "memory")
    const int sr = lane >> 3, spos = lane & 7;
    const unsigned voffB = ((sr & 3) + 16 * (sr >> 2)) * 2048 + ((spos ^ (((sr >> 1) & 1) << 2)) * 16);
    const unsigned voffA = lane * 16;
    const unsigned ldsA = lds0 + wid * 1024, ldsB = lds0 + 8192 + wid * 1024;
    const char* pa = (const char*)H + ((size_t)(pm * 8 + wid) * 112 + ND * ks) * 1024;
    const char* pb = (const char*)wd + (size_t)(ND * ks * 32 + 4 * (wid & 3)) * 2048 + pn * 256 + (wid >> 2) * 128;
#define K4_ISSUE() do { const unsigned so_ = slot_i * 16384; glds16s(pa, voffA, ldsA + so_); glds16s(pb, voffB, ldsB + so_); pa += 1024; pb += 32 * 2048; slot_i = (slot_i == 4) ? 0 : slot_i + 1; } while (0)
    const int r32 = lane & 31, hi = lane >> 5, fr = lane & 15;
    const unsigned aX0 = lds0 + (wm * 4 + (r32 >> 4)) * 1024 + fr * 64 + ((hi ^ ((-(fr >> 2)) & 3)) * 16);
    const int tq = (lane & 15) >> 2, tp = lane & 3;
    const unsigned aW0 = lds0 + 8192 + wn * 4096 + (8 * hi + tq) * 128 + (((tq >> 1) & 1) << 6) + ((lane >> 4) & 1) * 32 + tp * 8;
    f32x16 acc[2][2];
#pragma unroll
    for (int m = 0; m < 2; ++m)
#pragma unroll
        for (int n = 0; n < 2; ++n) acc[m][n] = (f32x16){};
    bf16x8 xp[2] = {}, xq[2] = {}; s16x4 wp[4] = {}, wq[4] = {};
    int slot_i = 0;
#pragma unroll
    for (int i = 0; i < 5; ++i) K4_ISSUE();
    int rs_i = wk;
    VW(6);
    G_RD(xp, wp, rs_i * 16384, 0);
#define G_STEP(W, I1, I2) do { \
        const unsigned so = rs_i * 16384; \
        rs_i += 2; rs_i = (rs_i >= 5) ? rs_i - 5 : rs_i; \
        const unsigned sn = rs_i * 16384; \
        G_RD(xq, wq, so, 1); SBAR(); G_MM(xp, wp); SBAR(); \
        VW(W); \
        G_RD(xp, wp, sn, 0); SBAR(); if (I1) K4_ISSUE(); if (I2) K4_ISSUE(); SBAR(); G_MM(xq, wq); SBAR(); } while (0)
#pragma nounroll
    for (int t = 0; t < ND / 2 - 3; ++t) { asm volatile("" : "+s"(rs_i), "+s"(slot_i)); G_STEP(2, true, true); }
    G_STEP(2, true, false);
    G_STEP(0, false, false);
    G_STEP(0, false, false);
    asm volatile("s_waitcnt lgkmcnt(0)\n\ts_barrier" ::: "memory");
    {
        int lz = threadIdx.x & 63; asm volatile("" : "+v"(lz));
        const int rr = lz & 31, hh = lz >> 5;
        f32x16 give0, give1, keep0, keep1;
        if (wk) { give0 = acc[0][0]; give1 = acc[0][1]; keep0 = acc[1][0]; keep1 = acc[1][1]; }
        else { give0 = acc[1][0]; give1 = acc[1][1]; keep0 = acc[0][0]; keep1 = acc[0][1]; }
        LAS f32x4* xs = (LAS f32x4*)(lds + ((wid & 3) * 2 + wk) * 8192) + lz;
#pragma unroll
        for (int g = 0; g < 4; ++g) {
            xs[g * 64] = (f32x4){give0[4 * g], give0[4 * g + 1], give0[4 * g + 2], give0[4 * g + 3]};
            xs[(4 + g) * 64] = (f32x4){give1[4 * g], give1[4 * g + 1], give1[4 * g + 2], give1[4 * g + 3]};
        }
        asm volatile("s_waitcnt lgkmcnt(0)\n\ts_barrier" ::: "memory");
        const LAS f32x4* xr = (const LAS f32x4*)(lds + ((wid & 3) * 2 + (1 - wk)) * 8192) + lz;
        const __amdgpu_buffer_rsrc_t rsP = __builtin_amdgcn_make_buffer_rsrc(P, 0, 8 << 20, 0x00020000);
        const unsigned pd = ((unsigned)(((ks * 16 + pm) * 8 + pn) * 8 + (wm * 2 + wk) * 2 + wn) * 256 + lz) * 16;
#pragma unroll
        for (int gp = 0; gp < 2; ++gp) {
            const int g = 2 * gp;
            const f32x4 a0 = (f32x4){keep0[4 * g], keep0[4 * g + 1], keep0[4 * g + 2], keep0[4 * g + 3]} + xr[g * 64];
            const f32x4 a1 = (f32x4){keep0[4 * g + 4], keep0[4 * g + 5], keep0[4 * g + 6], keep0[4 * g + 7]} + xr[(g + 1) * 64];
            const f32x4 b0 = (f32x4){keep1[4 * g], keep1[4 * g + 1], keep1[4 * g + 2], keep1[4 * g + 3]} + xr[(4 + g) * 64];
            const f32x4 b1 = (f32x4){keep1[4 * g + 4], keep1[4 * g + 5], keep1[4 * g + 6], keep1[4 * g + 7]} + xr[(4 + g + 1) * 64];
            const u32x4 wa = {cvtpk(a0[0], a0[1]), cvtpk(a0[2], a0[3]), cvtpk(a1[0], a1[1]), cvtpk(a1[2], a1[3])};
            const u32x4 wb = {cvtpk(b0[0], b0[1]), cvtpk(b0[2], b0[3]), cvtpk(b1[0], b1[1]), cvtpk(b1[2], b1[3])};
            __builtin_amdgcn_raw_buffer_store_b128(wa, rsP, pd + gp * 1024, 0, 16);
            __builtin_amdgcn_raw_buffer_store_b128(wb, rsP, pd + (2 + gp) * 1024, 0, 16);
        }
        asm volatile("s_waitcnt vmcnt(0)\n\ts_barrier" ::: "memory");
        if (threadIdx.x == 0) __hip_atomic_fetch_add(flags + (pm * 8 + pn) * 32, 1u, __ATOMIC_RELAXED, __HIP_MEMORY_SCOPE_AGENT);
        (void)rr; (void)hh;
    }
}

#define UNP(a, b, i) ((f32x4){__uint_as_float(a[i] << 16) + __uint_as_float(b[i] << 16), __uint_as_float(a[i] & 0xffff0000u) + __uint_as_float(b[i] & 0xffff0000u), \
                              __uint_as_float(a[(i) + 1] << 16) + __uint_as_float(b[(i) + 1] << 16), __uint_as_float(a[(i) + 1] & 0xffff0000u) + __uint_as_float(b[(i) + 1] & 0xffff0000u)})

__device__ __forceinline__ void wo_role(const u16* __restrict__ H, const u16* __restrict__ wd, const u16* __restrict__ Ob, const u16* __restrict__ wo, const float* __restrict__ P, const float* __restrict__ G, u16* __restrict__ out,
                                        unsigned* __restrict__ flags, unsigned* __restrict__ cnt, char* lds, int bid)
{
    int tid = threadIdx.x; asm volatile("" : "+v"(tid));
    const int lane = tid & 63;
    const int wid = __builtin_amdgcn_readfirstlane(tid >> 6);
    const int wm = wid & 1, wn = (wid >> 1) & 1, wk = wid >> 2;
    const int xcd = bid & 7, idx = bid >> 3;
    const int rt = 8 * (xcd >> 1) + (idx & 7), pn = 4 * (xcd & 1) + (idx >> 3);
    const unsigned lds0 = (unsigned)(uintptr_t)lds;
    const int sr = lane >> 3, spos = lane & 7;
    const unsigned voffB = ((sr & 3) + 16 * (sr >> 2)) * 2048 + ((spos ^ (((sr >> 1) & 1) << 2)) * 16);
    const unsigned voffA = lane * 16;
    const char* pa = (const char*)H + ((size_t)(rt * 4 + (wid & 3)) * 112 + (112 - EXH) + (wid >> 2)) * 1024;
    const char* pb = (const char*)wd + (size_t)((112 - EXH) * 32 + 4 * (wid & 3)) * 2048 + pn * 256 + (wid >> 2) * 128;
    const char* paO = (const char*)Ob + ((size_t)(rt * 4 + (wid & 3)) * 32 + (wid >> 2)) * 1024;
    const char* pbO = (const char*)wo + (size_t)(4 * (wid & 3)) * 2048 + pn * 256 + (wid >> 2) * 128;
    int left = EXH / 2, sl = 0, sc = 0;
    const unsigned dA = lds0 + (wid >> 2) * 12288 + (wid & 3) * 1024, dB = lds0 + 4096 + wid * 1024;
#define K5_ISSUE() do { const unsigned so_ = sl * 24576; glds16s(pa, voffA, dA + so_); glds16s(pb, voffB, dB + so_); glds16s(pb + 32 * 2048, voffB, dB + so_ + 12288); \
                        pa += 2048; pb += (size_t)64 * 2048; sl = (sl == 2) ? 0 : sl + 1; --left; pa = (left == 0) ? paO : pa; pb = (left == 0) ? pbO : pb; } while (0)
    float gmv;
    { const unsigned go = (unsigned)(rt * 64 + wm * 32 + (lane & 31)) * 4; asm volatile("global_load_dword %0, %1, %2" : "=v"(gmv) : "v"(go), "s"(G) : "memory"); }
    K5_ISSUE(); K5_ISSUE(); K5_ISSUE();
    const int r32 = lane & 31, hi = lane >> 5, fr = lane & 15;
    const unsigned aX0 = lds0 + wk * 12288 + (wm * 2 + (r32 >> 4)) * 1024 + fr * 64 + ((hi ^ ((-(fr >> 2)) & 3)) * 16);
    const int tq = (lane & 15) >> 2, tp = lane & 3;
    const unsigned aW0 = lds0 + wk * 12288 + 4096 + wn * 4096 + (8 * hi + tq) * 128 + (((tq >> 1) & 1) << 6) + ((lane >> 4) & 1) * 32 + tp * 8;
    f32x16 acc[2] = {};
#define K5_COMP() do { const unsigned so = sc * 24576; \
        const bf16x8 x0 = ldsb128(LP(aX0 + so)), x1 = ldsb128(LP((aX0 + so) ^ 32)); \
        const s16x4 w0 = vtr(LP(aW0 + so)), w1 = vtr(LP(aW0 + so) + 512), w2 = vtr(LP((aW0 + so) ^ 64)), w3 = vtr(LP((aW0 + so) ^ 64) + 512); \
        const s16x4 w4 = vtr(LP(aW0 + so) + 2048), w5 = vtr(LP(aW0 + so) + 2560), w6 = vtr(LP((aW0 + so) ^ 64) + 2048), w7 = vtr(LP((aW0 + so) ^ 64) + 2560); \
        acc[0] = MFMA32(cat8(w0, w1), x0, acc[0]); acc[1] = MFMA32(cat8(w2, w3), x0, acc[1]); acc[0] = MFMA32(cat8(w4, w5), x1, acc[0]); acc[1] = MFMA32(cat8(w6, w7), x1, acc[1]); \
        sc = (sc == 2) ? 0 : sc + 1; } while (0)
#define K5_RUN(n_) do { _Pragma("nounroll") for (int s2 = 0; s2 < (n_); ++s2) { asm volatile("" : "+s"(sl), "+s"(sc), "+s"(left)); VW(3); K5_ISSUE(); K5_COMP(); } } while (0)
    VW(6); asm volatile("" : "+v"(gmv)); gmv = 1.0f - gmv; K5_COMP();
    K5_RUN(EXH / 2 - 3);
    if (wid == 0) {
        unsigned spins = 0;
        while (__builtin_amdgcn_readfirstlane(__hip_atomic_load(cnt + (rt >> 2) * 32, __ATOMIC_RELAXED, __HIP_MEMORY_SCOPE_AGENT)) < 32u) { __builtin_amdgcn_s_sleep(2); if (++spins > (1u << 22)) break; }
        __builtin_amdgcn_fence(__ATOMIC_ACQUIRE, "agent");
        asm volatile("s_waitcnt vmcnt(0)" ::: "memory");
    }
    K5_RUN(2);
    {
        acc[0] *= gmv; acc[1] *= gmv;
    }
    K5_RUN(13);
    if (wid == 0) {
        unsigned spins = 0;
        while (__builtin_amdgcn_readfirstlane(__hip_atomic_load(flags + ((rt >> 1) * 8 + pn) * 32, __ATOMIC_RELAXED, __HIP_MEMORY_SCOPE_AGENT)) < 2u) { __builtin_amdgcn_s_sleep(1); if (++spins > (1u << 22)) break; }
    }
    int lz = threadIdx.x & 63; asm volatile("" : "+v"(lz));
    const int rr = lz & 31, hh = lz >> 5;
    const __amdgpu_buffer_rsrc_t rsP = __builtin_amdgcn_make_buffer_rsrc((void*)P, 0, 8 << 20, 0x00020000);
    const unsigned blk = (unsigned)(((rt >> 1) * 8 + pn) * 8 + ((rt & 1) * 2 + wm) * 2 + wn);
    const unsigned p0 = (blk * 256 + wk * 128 + lz) * 16, p1 = p0 + 16 * 8 * 8 * 256 * 16;
    u32x4 pv[4];
    VW(3); K5_ISSUE();
    pv[0] = __builtin_amdgcn_raw_buffer_load_b128(rsP, p0, 0, 16); pv[1] = __builtin_amdgcn_raw_buffer_load_b128(rsP, p0 + 1024, 0, 16);
    pv[2] = __builtin_amdgcn_raw_buffer_load_b128(rsP, p1, 0, 16); pv[3] = __builtin_amdgcn_raw_buffer_load_b128(rsP, p1 + 1024, 0, 16);
    asm volatile("" ::: "memory");
    K5_COMP();
    VW(7); K5_COMP();
    VW(4); K5_COMP();
    f32x16 give, keep; if (wk) { give = acc[0]; keep = acc[1]; } else { give = acc[1]; keep = acc[0]; }
    asm volatile("s_waitcnt lgkmcnt(0)\n\ts_barrier" ::: "memory");
    const float gm = gmv;
    LAS f32x4* xs = (LAS f32x4*)(lds + ((wid & 3) * 2 + wk) * 4096) + lz;
#pragma unroll
    for (int g = 0; g < 4; ++g) xs[g * 64] = (f32x4){give[4 * g], give[4 * g + 1], give[4 * g + 2], give[4 * g + 3]};
    asm volatile("s_waitcnt lgkmcnt(0)\n\ts_barrier" ::: "memory");
    const LAS f32x4* xr = (const LAS f32x4*)(lds + ((wid & 3) * 2 + (1 - wk)) * 4096) + lz;
    const int row = rt * 64 + wm * 32 + rr;
#pragma unroll
    for (int g = 0; g < 4; g += 2) {
        const f32x4 v0 = (f32x4){keep[4 * g], keep[4 * g + 1], keep[4 * g + 2], keep[4 * g + 3]} + xr[g * 64] + UNP(pv[g >> 1], pv[2 + (g >> 1)], 0) * gm;
        const f32x4 v1 = (f32x4){keep[4 * g + 4], keep[4 * g + 5], keep[4 * g + 6], keep[4 * g + 7]} + xr[(g + 1) * 64] + UNP(pv[g >> 1], pv[2 + (g >> 1)], 2) * gm;
        const unsigned ax = cvtpk(v0[0], v0[1]), ay = cvtpk(v0[2], v0[3]), bx = cvtpk(v1[0], v1[1]), by = cvtpk(v1[2], v1[3]);
        auto rx = __builtin_amdgcn_permlane32_swap(ax, bx, false, false); auto ry = __builtin_amdgcn_permlane32_swap(ay, by, false, false);
        const u32x4 w = {rx[0], ry[0], rx[1], ry[1]};
        const int col = pn * 128 + wn * 64 + wk * 32 + (g + hh) * 8;
        *(u32x4*)(out + (size_t)row * 1024 + col) = w;
    }
}

__global__ __launch_bounds__(512, 4) void k23_fused(const u16* __restrict__ QKV, const float* __restrict__ KMAX, const float* __restrict__ G, u16* __restrict__ O,
                                                    const u16* __restrict__ H, const u16* __restrict__ wd, const u16* __restrict__ wo, u16* __restrict__ out, float* __restrict__ P,
                                                    unsigned* __restrict__ flags)
{
    extern __shared__ __attribute__((aligned(16))) char lds[];
    const int bid = blockIdx.x;
    unsigned* cnt = flags + 256 * 32;
    if (bid < 256) { attn_role(QKV, KMAX, G, O, cnt, lds, bid); wo_role(H, wd, O, wo, P, G, out, flags, cnt, lds, bid); }
    else gemm_role(H, wd, P, flags, lds, bid);
}

extern "C" void kernel_launch(void* const* d_in, const int* in_sizes, int n_in, void* d_out, int out_size, void* d_ws, size_t ws_size, hipStream_t stream)
{
    const u16* x = (const u16*)d_in[0]; const u16* wqkv = (const u16*)d_in[1]; const u16* wo = (const u16*)d_in[2];
    const u16* wg = (const u16*)d_in[4]; const u16* wu = (const u16*)d_in[5]; const u16* wd = (const u16*)d_in[6]; const u16* gw = (const u16*)d_in[7];
    char* ws = (char*)d_ws;
    u16* QKV = (u16*)ws;
    u16* H = (u16*)(ws + (16u << 20));
    u16* Ob = (u16*)(ws + (32u << 20));
    float* P = (float*)(ws + (40u << 20));
    float* G = (float*)(ws + (48u << 20));
    float* KM = (float*)(ws + (48u << 20) + 65536);
    unsigned* flags = (unsigned*)(ws + (48u << 20) + 131072);
    hipFuncSetAttribute((const void*)k1_gemm, hipFuncAttributeMaxDynamicSharedMemorySize, K1_LDS);
    hipFuncSetAttribute((const void*)k23_fused, hipFuncAttributeMaxDynamicSharedMemorySize, K23_LDS);
    hipLaunchKernelGGL(k1_gemm, dim3(256), dim3(512), K1_LDS, stream, x, wqkv, wg, wu, gw, QKV, H, G, KM, flags, wd, wo);
    hipLaunchKernelGGL(k23_fused, dim3(512), dim3(512), K23_LDS, stream, QKV, KM, G, Ob, H, wd, wo, (u16*)d_out, P, flags);
}
```
